```python
import torch

def _softmax(x, axis=-1):
    return torch.softmax(x, dim=axis)
def _gelu(x):
    return torch.nn.functional.gelu(x, approximate="tanh")
def _silu(x):
    return torch.nn.functional.silu(x)
def _sigmoid(x):
    return torch.sigmoid(x)
def _layernorm(x, axis=-1, eps=1e-5):
    m = x.mean(dim=axis, keepdim=True)
    v = x.var(dim=axis, keepdim=True, unbiased=False)
    return (x - m) / torch.sqrt(v + eps)
def _scatter_add(tgt, idx, src, axis=0):
    return tgt.clone().index_add_(axis, idx, src)
def _segment_sum(src, seg, n):
    out = torch.zeros((n,) + src.shape[1:], dtype=src.dtype)
    return out.index_add_(0, seg, src)
def _pad(x, pw):
    flat = tuple(v for pair in reversed(pw) for v in pair)
    return torch.nn.functional.pad(x, flat)

kernel_name = 'sequential_preLN__ssm_conv_s4d__glu_gated_reglu__prompt'

def setup_inputs():

    _w = (lambda a: (lambda t: t.to(torch.bfloat16) if t.is_floating_point() else t)(torch.as_tensor(a)))
    def _A_setup():
        Bsz, L, D, N = 1, 2048, 512, 128
        return {
            "u": _w((lambda *s: torch.randn(*s, dtype=torch.bfloat16) * 0.05)(Bsz, L, D)),
            "log_A_real": _w((lambda *s: torch.randn(*s, dtype=torch.bfloat16) * 6.4)(D, N) * 0.5),
            "A_imag": _w((lambda *s: torch.randn(*s, dtype=torch.bfloat16) * 0.05)(D, N) * 3.14),
            "C_real": _w((lambda *s: torch.randn(*s, dtype=torch.bfloat16) * 0.05)(D, N) / N ** 0.5),
            "C_imag": _w((lambda *s: torch.randn(*s, dtype=torch.bfloat16) * 0.05)(D, N) / N ** 0.5),
            "log_dt": _w((lambda *s: torch.randn(*s, dtype=torch.bfloat16) * 0.05)(D) * 0.5 - 4.0),
            "D_skip": _w((lambda *s: torch.randn(*s, dtype=torch.bfloat16) * 0.05)(D)),
        }
    _A_in = _A_setup()

    def _B_setup():
        B, S, D, F = 1, 2048, 512, 1376
        return {
            "x": _w((lambda *s: torch.randn(*s, dtype=torch.bfloat16) * 0.05)(B, S, D)),
            "W_gate": _w((lambda *s: torch.randn(*s, dtype=torch.bfloat16) * 0.4)(D, F) * (2.0 / D) ** 0.5),
            "W_up": _w((lambda *s: torch.randn(*s, dtype=torch.bfloat16) * 0.4)(D, F) * (2.0 / D) ** 0.5),
            "W_down": _w((lambda *s: torch.randn(*s, dtype=torch.bfloat16) * 0.4)(F, D) * (1.0 / F) ** 0.5),
        }
    _B_in = _B_setup()

    return {
        "A_u": _A_in["u"],
        "A_log_A_real": _A_in["log_A_real"],
        "A_A_imag": _A_in["A_imag"],
        "A_C_real": _A_in["C_real"],
        "A_C_imag": _A_in["C_imag"],
        "A_log_dt": _A_in["log_dt"],
        "A_D_skip": _A_in["D_skip"],
        "B_x": _B_in["x"],
        "B_W_gate": _B_in["W_gate"],
        "B_W_up": _B_in["W_up"],
        "B_W_down": _B_in["W_down"],
    }

def reference(A_u, A_log_A_real, A_A_imag, A_C_real, A_C_imag, A_log_dt, A_D_skip, B_x, B_W_gate, B_W_up, B_W_down):
    def _A_ref(u, log_A_real, A_imag, C_real, C_imag, log_dt, D_skip):
        L = u.shape[1]
        dt = torch.exp(log_dt)
        A = -torch.exp(log_A_real) + 1j * A_imag
        C = (C_real + 1j * C_imag) * dt[:, None]
        pos = (lambda x, dt: x.to(dt))(torch.arange(L), log_dt.dtype)
        P = torch.exp((A * dt[:, None])[:, :, None] * pos[None, None, :])
        K = 2.0 * torch.einsum("dn,dnl->dl", C, P).real
        L2 = 2 * L
        u_t = (lambda x, *d: x.permute(*d))(u, 0, 2, 1)
        Kf = (lambda x, *a, **k: torch.fft.rfft(x.float(), *a, **k))(K, L2)
        Uf = (lambda x, *a, **k: torch.fft.rfft(x.float(), *a, **k))(u_t, L2)
        y = (lambda x, *a, **k: torch.fft.irfft(x, *a, **k).to(torch.bfloat16))(Uf * Kf[None, :, :], L2)[:, :, :L]
        y = (lambda x, *d: x.permute(*d))(y, 0, 2, 1)
        return y + u * D_skip[None, None, :]

    def _B_ref(x, W_gate, W_up, W_down):
        gate = torch.einsum("bsd,df->bsf", x, W_gate)
        up = torch.einsum("bsd,df->bsf", x, W_up)
        gate = gate * (lambda x, dt: x.to(dt))(gate > 0, gate.dtype)
        h = gate * up
        return torch.einsum("bsf,fd->bsd", h, W_down)

    x = A_u
    h = x + _A_ref(_layernorm(x, axis=-1), A_log_A_real, A_A_imag, A_C_real, A_C_imag, A_log_dt, A_D_skip)
    h2 = h + _B_ref(_layernorm(h, axis=-1), B_W_gate, B_W_up, B_W_down)
    return h2
```

```cpp
#include <hip/hip_runtime.h>
#include <hip/hip_bf16.h>
#include <stdint.h>

typedef unsigned short u16;
static constexpr int L = 2048, D = 512, NM = 128, F = 1376;

__device__ __forceinline__ float bf2f(u16 v) { return __uint_as_float(((unsigned)v) << 16); }
__device__ __forceinline__ u16 f2bf(float f) {
  unsigned u = __float_as_uint(f);
  if ((u & 0x7fffffffu) > 0x7f800000u) return (u16)((u >> 16) | 0x40);
  return (u16)((u + 0x7FFFu + ((u >> 16) & 1u)) >> 16);
}
__device__ __forceinline__ float rbf(float f) { return bf2f(f2bf(f)); }

__global__ void k_ln(const u16* __restrict__ x, u16* __restrict__ out) {
  __shared__ float red[256];
  int row = blockIdx.x, t = threadIdx.x;
  float a = bf2f(x[row * D + t]), b = bf2f(x[row * D + t + 256]);
  red[t] = a + b; __syncthreads();
  for (int s = 128; s > 0; s >>= 1) { if (t < s) red[t] += red[t + s]; __syncthreads(); }
  float mean = red[0] / D; __syncthreads();
  float da = a - mean, db = b - mean;
  red[t] = da * da + db * db; __syncthreads();
  for (int s = 128; s > 0; s >>= 1) { if (t < s) red[t] += red[t + s]; __syncthreads(); }
  float var = red[0] / D;
  float m = rbf(mean), v = rbf(var);
  float den = rbf(sqrtf(rbf(v + 1e-5f)));
  out[row * D + t] = f2bf(rbf(a - m) / den);
  out[row * D + t + 256] = f2bf(rbf(b - m) / den);
}

__global__ void k_kgen(const u16* lar, const u16* aim, const u16* cr, const u16* ci, const u16* ldt, float* K) {
  int d = blockIdx.y; int l = blockIdx.x * 256 + threadIdx.x;
  float dt = rbf(expf(bf2f(ldt[d])));
  float p = rbf((float)l);
  float acc = 0.f;
  for (int n = 0; n < NM; ++n) {
    float ar = -rbf(expf(bf2f(lar[d * NM + n])));
    float ai = bf2f(aim[d * NM + n]);
    float zr = (ar * dt) * p, zi = (ai * dt) * p;
    float e = expf(zr);
    float c_r = bf2f(cr[d * NM + n]) * dt, c_i = bf2f(ci[d * NM + n]) * dt;
    float s, c; sincosf(zi, &s, &c);
    acc += e * (c_r * c - c_i * s);
  }
  K[d * L + l] = 2.f * acc;
}

__global__ void k_conv(const u16* x, const u16* un, const float* K, const u16* dsk, u16* h) {
  int d = blockIdx.x * 64 + (threadIdx.x & 63);
  int l = blockIdx.y * 4 + (threadIdx.x >> 6);
  float acc = 0.f;
  for (int m = 0; m <= l; ++m) acc += K[d * L + m] * bf2f(un[(l - m) * D + d]);
  float y = rbf(acc);
  float sk = rbf(bf2f(un[l * D + d]) * bf2f(dsk[d]));
  float a = rbf(y + sk);
  h[l * D + d] = f2bf(bf2f(x[l * D + d]) + a);
}

__global__ void k_gemm1(const u16* xn, const u16* wg, const u16* wu, u16* H) {
  int f = blockIdx.x * 32 + (threadIdx.x & 31);
  int l = blockIdx.y * 8 + (threadIdx.x >> 5);
  float g = 0.f, u = 0.f;
  for (int k = 0; k < D; ++k) { float a = bf2f(xn[l * D + k]); g += a * bf2f(wg[k * F + f]); u += a * bf2f(wu[k * F + f]); }
  g = rbf(g); u = rbf(u);
  g = g * (g > 0.f ? 1.f : 0.f);
  H[l * F + f] = f2bf(g * u);
}
__global__ void k_gemm2(const u16* H, const u16* wd, const u16* h, u16* out) {
  int d = blockIdx.x * 64 + (threadIdx.x & 63);
  int l = blockIdx.y * 4 + (threadIdx.x >> 6);
  float a = 0.f;
  for (int k = 0; k < F; ++k) a += bf2f(H[l * F + k]) * bf2f(wd[k * D + d]);
  out[l * D + d] = f2bf(bf2f(h[l * D + d]) + rbf(a));
}

extern "C" void kernel_launch(void* const* d_in, const int* in_sizes, int n_in,
                              void* d_out, int out_size, void* d_ws, size_t ws_size,
                              hipStream_t stream) {
  const u16* u = (const u16*)d_in[0];
  const u16* lar = (const u16*)d_in[1];
  const u16* aim = (const u16*)d_in[2];
  const u16* cr = (const u16*)d_in[3];
  const u16* ci = (const u16*)d_in[4];
  const u16* ldt = (const u16*)d_in[5];
  const u16* dsk = (const u16*)d_in[6];
  const u16* wg = (const u16*)d_in[8];
  const u16* wu = (const u16*)d_in[9];
  const u16* wd = (const u16*)d_in[10];
  char* ws = (char*)d_ws;
  u16* un = (u16*)ws;
  float* K = (float*)(ws + (2 << 20));
  u16* h = (u16*)(ws + (6 << 20));
  u16* xn2 = (u16*)(ws + (8 << 20));
  u16* H = (u16*)(ws + (10 << 20));
  k_ln<<<L, 256, 0, stream>>>(u, un);
  k_kgen<<<dim3(L / 256, D), 256, 0, stream>>>(lar, aim, cr, ci, ldt, K);
  k_conv<<<dim3(D / 64, L / 4), 256, 0, stream>>>(u, un, K, dsk, h);
  k_ln<<<L, 256, 0, stream>>>(h, xn2);
  k_gemm1<<<dim3(F / 32, L / 8), 256, 0, stream>>>(xn2, wg, wu, H);
  k_gemm2<<<dim3(D / 64, L / 4), 256, 0, stream>>>(H, wd, h, (u16*)d_out);
}
```

```cpp
#include <hip/hip_runtime.h>
#include <hip/hip_bf16.h>
#include <stdint.h>

typedef unsigned short u16;
typedef unsigned int u32;
typedef unsigned long long u64;
typedef _Float16 h16x8 __attribute__((ext_vector_type(8)));
typedef _Float16 h16x2 __attribute__((ext_vector_type(2)));
typedef __bf16 b16x8 __attribute__((ext_vector_type(8)));
typedef __bf16 b16x2 __attribute__((ext_vector_type(2)));
typedef short s16x4 __attribute__((ext_vector_type(4)));
typedef float f32x4 __attribute__((ext_vector_type(4)));
typedef float f32x2 __attribute__((ext_vector_type(2)));
typedef u32 u32x4 __attribute__((ext_vector_type(4)));
typedef u32 u32x2 __attribute__((ext_vector_type(2)));
#define LAS __attribute__((address_space(3)))
#define GAS __attribute__((address_space(1)))

static constexpr int L = 2048, D = 512, NM = 128, F = 1376;
static constexpr int NWG = 256, NT = 512;

static constexpr u32 WS_GEN = 0;
static constexpr u32 WS_FLAGH = 8192;
static constexpr u32 WS_STATS = 16384;
static constexpr u32 WS_FLAGB = 32768;
static constexpr u32 WS_FLAGW = 49152;
static constexpr u32 WS_HT = 65536;
static constexpr u32 WS_PB = WS_HT + D * L * 2;
static constexpr u32 WS_SB = WS_PB + 28 * 128 * 1024;
static constexpr u32 WS_WD = WS_SB + 30 * 64 * 1024;
static constexpr u32 XTS = 16384 + 128;
static constexpr u32 WS_XT = WS_WD + 43 * 32 * 1024;
static constexpr u32 WS_WT = WS_XT + 256 * XTS;
static constexpr u32 WS_DBG = WS_WT + 29 * 98304;
static constexpr u32 WS_END = WS_DBG + (1u << 20);

static constexpr int XROW = 1056;
static constexpr int LDS_TAB = 0;
static constexpr int TABCH = 57 * XROW;
static constexpr int LDS_W = 0;
static constexpr int LDS_F = 98304;
static constexpr int FCOPY = 4160;
static constexpr int LDS_PART = 131584;
static constexpr int LDS_STG = 120832;
static constexpr int LDS_KC = 141824;
static constexpr int KCCH = 2944;
static constexpr int LDS_U = 150784;
static constexpr int UCH = 2288 * 2;
static constexpr int LDS_CS = 160000;
static constexpr int LDS_SC = 160384;
static constexpr int LDS_CNT = 160400;
static constexpr int LDS_DBG = 160768;
static constexpr int LDS_RINGP = 98304;
static constexpr int LDS_RINGQ = 131072;
static constexpr int LDS_P2 = 0;
static constexpr int LDS_RES = 131072;
static constexpr int LDS_TOTAL = 163840;

struct Params {
  const u16* x; const u16* lar; const u16* aim; const u16* cr; const u16* ci; const u16* ldt; const u16* dsk;
  const u16* wg; const u16* wu; const u16* wd; u16* out; char* ws;
};

__device__ __forceinline__ float bf2f(u32 v) { return __uint_as_float(v << 16); }
__device__ __forceinline__ float bfhi(u32 v) { return __uint_as_float(v & 0xffff0000u); }
__device__ __forceinline__ u32 cvt2(float lo, float hi) {
  f32x2 v; v.x = lo; v.y = hi;
  b16x2 r = __builtin_convertvector(v, b16x2);
  return __builtin_bit_cast(u32, r);
}
__device__ __forceinline__ float rbf(float f) { return bf2f(cvt2(f, f) & 0xffffu); }

__device__ __forceinline__ f32x2 cmul(f32x2 a, f32x2 b) {
  f32x2 t, r;
  asm("v_pk_mul_f32 %0, %1, %2 op_sel_hi:[0,1]" : "=v"(t) : "v"(a), "v"(b));
  asm("v_pk_fma_f32 %0, %1, %2, %3 op_sel:[1,1,0] op_sel_hi:[1,0,1] neg_lo:[0,1,0]" : "=v"(r) : "v"(a), "v"(b), "v"(t));
  return r;
}
__device__ __forceinline__ f32x2 cexp_k(float ar2, float airev, float sc) {
  const float e = __builtin_amdgcn_exp2f(ar2 * sc);
  const float rev = __builtin_amdgcn_fractf(airev * sc);
  f32x2 o; o.x = __builtin_amdgcn_cosf(rev); o.y = __builtin_amdgcn_sinf(rev);
  return o * e;
}
__device__ __forceinline__ float fast_expf(float x) {
  const float t = x * 1.44269504088896341f;
  const float r = __builtin_fmaf(x, 1.44269504088896341f, -t) + x * 1.925963033500e-08f;
  return __builtin_amdgcn_exp2f(t) * __builtin_fmaf(r, 0.6931471805599453f, 1.0f);
}

#define BARRIER() do { asm volatile("s_waitcnt lgkmcnt(0)" ::: "memory"); __builtin_amdgcn_s_barrier(); asm volatile("" ::: "memory"); } while (0)
#define WAIT_VM(n) asm volatile("s_waitcnt vmcnt(" #n ")" ::: "memory")
static constexpr u32 SPIN_CAP = 1u << 17;

__device__ __forceinline__ __amdgpu_buffer_rsrc_t mk_rsrc(const void* p, u32 bytes) {
  return __builtin_amdgcn_make_buffer_rsrc((void*)p, 0, bytes, 0x00020000);
}
template <int CTRL, int RM>
__device__ __forceinline__ float dpp_addf(float v) {
  return v + __int_as_float(__builtin_amdgcn_update_dpp(0, __float_as_int(v), CTRL, RM, 0xf, true));
}
__device__ __forceinline__ float wave_sum(float v) {
  v = dpp_addf<0x111, 0xf>(v); v = dpp_addf<0x112, 0xf>(v); v = dpp_addf<0x114, 0xf>(v); v = dpp_addf<0x118, 0xf>(v);
  v = dpp_addf<0x142, 0xa>(v); v = dpp_addf<0x143, 0xc>(v);
  return __int_as_float(__builtin_amdgcn_readlane(__float_as_int(v), 63));
}
template <int CTRL, int RM>
__device__ __forceinline__ u32 dpp_maxu(u32 v) {
  u32 o = (u32)__builtin_amdgcn_update_dpp(0, (int)v, CTRL, RM, 0xf, true);
  return v > o ? v : o;
}
__device__ __forceinline__ u32 wave_maxu(u32 v) {
  v = dpp_maxu<0x111, 0xf>(v); v = dpp_maxu<0x112, 0xf>(v); v = dpp_maxu<0x114, 0xf>(v); v = dpp_maxu<0x118, 0xf>(v);
  v = dpp_maxu<0x142, 0xa>(v); v = dpp_maxu<0x143, 0xc>(v);
  return (u32)__builtin_amdgcn_readlane((int)v, 63);
}
__device__ __forceinline__ float sum_rows(float v) {
  auto a = __builtin_amdgcn_permlane16_swap(__float_as_uint(v), __float_as_uint(v), false, false);
  v = __uint_as_float(a[0]) + __uint_as_float(a[1]);
  auto c = __builtin_amdgcn_permlane32_swap(__float_as_uint(v), __float_as_uint(v), false, false);
  return __uint_as_float(c[0]) + __uint_as_float(c[1]);
}
template <bool NEGIM>
__device__ __forceinline__ void put_tab(char* smem, int off, f32x2 v) {
  u32x2 o;
  const float re = v.x, im = v.y;
  if constexpr (NEGIM) {
    asm("v_cvt_pkrtz_f16_f32 %0, %1, -%2" : "=v"(o.x) : "v"(re), "v"(im));
    asm("v_fma_mixlo_f16 %0, -%1, 1.0, %2 op_sel_hi:[1,0,0]" : "=v"(o.y) : "v"(o.x), "v"(re));
    asm("v_fma_mixhi_f16 %0, -%1, 1.0, -%2 op_sel:[1,0,0] op_sel_hi:[1,0,0]" : "+v"(o.y) : "v"(o.x), "v"(im));
  } else {
    asm("v_cvt_pkrtz_f16_f32 %0, %1, %2" : "=v"(o.x) : "v"(re), "v"(im));
    asm("v_fma_mixlo_f16 %0, -%1, 1.0, %2 op_sel_hi:[1,0,0]" : "=v"(o.y) : "v"(o.x), "v"(re));
    asm("v_fma_mixhi_f16 %0, -%1, 1.0, %2 op_sel:[1,0,0] op_sel_hi:[1,0,0]" : "+v"(o.y) : "v"(o.x), "v"(im));
  }
  *(u32x2*)(smem + off) = o;
}
__device__ __forceinline__ s16x4 tr_read(char* smem, int off) {
  return __builtin_amdgcn_ds_read_tr16_b64_v4i16((LAS s16x4*)(smem + off));
}
__device__ __forceinline__ b16x8 mkfrag(s16x4 a, s16x4 b) {
  typedef short s16x8 __attribute__((ext_vector_type(8)));
  s16x8 v = __builtin_shufflevector(a, b, 0, 1, 2, 3, 4, 5, 6, 7);
  return __builtin_bit_cast(b16x8, v);
}
template <bool COH>
__device__ __forceinline__ void glds16(const void* sbase, u32 voff, u32 lds_dst) {
  u32 keep;
  if constexpr (COH)
    asm volatile("s_mov_b32 %0, m0\n\ts_mov_b32 m0, %3\n\ts_nop 0\n\tglobal_load_lds_dwordx4 %1, %2 sc1\n\ts_mov_b32 m0, %0"
                 : "=&s"(keep) : "v"(voff), "s"(sbase), "s"(lds_dst) : "memory");
  else
    asm volatile("s_mov_b32 %0, m0\n\ts_mov_b32 m0, %3\n\ts_nop 0\n\tglobal_load_lds_dwordx4 %1, %2\n\ts_mov_b32 m0, %0"
                 : "=&s"(keep) : "v"(voff), "s"(sbase), "s"(lds_dst) : "memory");
}
__device__ __forceinline__ void wait_flags256(__amdgpu_buffer_rsrc_t rs, u32 off, u32 tag, int lane) {
  for (u32 spins = 0; spins < SPIN_CAP; ++spins) {
    asm volatile("" ::: "memory");
    u32x4 v = __builtin_amdgcn_raw_buffer_load_b128(rs, off + lane * 16, 0, 16);
    bool ok = v.x == tag && v.y == tag && v.z == tag && v.w == tag;
    if (__all(ok)) break;
    __builtin_amdgcn_s_sleep(4);
  }
  asm volatile("" ::: "memory");
}
__device__ __forceinline__ u32 lds_inc(char* smem, int off, int lane) {
  asm volatile("s_waitcnt lgkmcnt(0)" ::: "memory");
  u32 old = 0;
  if (lane == 0) old = __hip_atomic_fetch_add((LAS u32*)(smem + off), 1u, __ATOMIC_RELAXED, __HIP_MEMORY_SCOPE_WORKGROUP);
  return (u32)__builtin_amdgcn_readfirstlane((int)old);
}
__device__ __forceinline__ void lds_wait(char* smem, int off, u32 n) {
  for (u32 spins = 0; spins < (SPIN_CAP << 3); ++spins) {
    asm volatile("" ::: "memory");
    u32 v = __hip_atomic_load((LAS u32*)(smem + off), __ATOMIC_RELAXED, __HIP_MEMORY_SCOPE_WORKGROUP);
    if ((u32)__builtin_amdgcn_readfirstlane(v) >= n) break;
    __builtin_amdgcn_s_sleep(1);
  }
  asm volatile("" ::: "memory");
}
__device__ __forceinline__ void publish8(char* ws, u32 off, int b, u32 tag, int lane) {
  if (lane < 8) __hip_atomic_store((GAS u32*)(ws + off + lane * 1024) + b, tag, __ATOMIC_RELAXED, __HIP_MEMORY_SCOPE_AGENT);
}

#define STAMP(i) do {} while (0)

template <int C0, int NF>
__device__ __forceinline__ void conv_part(char* smem, int fbase, int gbase, int rh, f32x4 (&acc)[4]) {
#pragma unroll
  for (int c = 0; c < 4; ++c) acc[c] = f32x4{0.f, 0.f, 0.f, 0.f};
  h16x8 Ff[2][NF], Gf[2][NF];
  auto load = [&](int rr, int buf) {
    const int rho = 4 * rh + rr;
    const int fa = fbase - 64 * rho, ga = gbase - 64 * rho;
#pragma unroll
    for (int e = 0; e < NF; ++e) {
      Gf[buf][e] = *(const h16x8*)(smem + ga + 512 * e);
      u32x2 lo = *(const u32x2*)(smem + fa + 512 * (7 - e)), hi = *(const u32x2*)(smem + fa + 512 * (7 - e) + 8);
      u32x4 v; v.x = lo.x; v.y = lo.y; v.z = hi.x; v.w = hi.y;
      Ff[buf][e] = __builtin_bit_cast(h16x8, v);
    }
  };
  load(0, 0);
#pragma unroll
  for (int rr = 0; rr < 4; ++rr) {
    if (rr < 3) load(rr + 1, (rr + 1) & 1);
#pragma unroll
    for (int c = 0; c < 4; ++c)
#pragma unroll
      for (int qq = 0; qq <= C0 + c; ++qq)
        acc[c] = __builtin_amdgcn_mfma_f32_16x16x32_f16(Ff[rr & 1][qq], Gf[rr & 1][C0 + c - qq], acc[c], 0, 0, 0);
  }
}

__global__ __launch_bounds__(NT, 2) void mega(Params p) {
  extern __shared__ __attribute__((aligned(16))) char smem[];
  const int tid = threadIdx.x, lane = tid & 63;
  const int w = __builtin_amdgcn_readfirstlane(tid >> 6);
  const int b = blockIdx.x, xcd = b & 7, slot = b >> 3;
  const int lr = lane & 15, lg = lane >> 4;
  const int tq = (lane & 15) >> 2, tp = lane & 3;
  const u32 lbase = (u32)(size_t)(LAS char*)smem;
  const __amdgpu_buffer_rsrc_t rs = mk_rsrc(p.ws, WS_END);
  const int ch0 = 64 * xcd + 2 * slot;
  const int pg = xcd >> 1, fh = xcd & 1, pi = slot & 1, sl = slot >> 1;
  const bool c_active = sl < 15 - fh;
  const int slice = 15 * fh + sl;
  const bool isQ = w >= 4;
  STAMP(0);
  if (tid < 16) *(u32*)(smem + LDS_CNT + tid * 4) = 0u;

  const u32 gen = __hip_atomic_load((GAS u32*)(p.ws + WS_GEN), __ATOMIC_RELAXED, __HIP_MEMORY_SCOPE_AGENT);
  const u32x4 xrow = *(const u32x4*)(p.x + (8 * b + w) * D + 8 * lane);
  u32 xt[4];
  {
    const u16* xs = p.x + (8 * b + 2 * (tid & 3)) * D + 2 * (tid >> 2);
    xt[0] = *(const u32*)(xs); xt[1] = *(const u32*)(xs + D); xt[2] = *(const u32*)(xs + 256); xt[3] = *(const u32*)(xs + 256 + D);
  }
  const int cc = w >> 2, part = (w >> 1) & 1, n = ((w & 1) << 6) | lane;
  const int d = ch0 + cc;
  const u32 v_lar = p.lar[d * NM + n], v_aim = p.aim[d * NM + n], v_cr = p.cr[d * NM + n], v_ci = p.ci[d * NM + n];
  const u32 cr2 = ((const u32*)(p.cr + d * NM))[lane], ci2 = ((const u32*)(p.ci + d * NM))[lane];
  const u32 v_ldt = p.ldt[d];
  const u32 dsk2 = *(const u32*)(p.dsk + ch0);
  const u32 tag = gen + 1u;
  {
    f32x2 v[4];
#pragma unroll
    for (int i = 0; i < 4; ++i) { const u32 t = xrow[i]; v[i].x = bf2f(t & 0xffffu); v[i].y = bfhi(t); }
    asm volatile("" :: "v"(v_lar), "v"(v_aim), "v"(v_cr), "v"(v_ci), "v"(cr2), "v"(ci2), "v"(v_ldt), "v"(dsk2), "v"(tag), "v"(xt[0]), "v"(xt[1]), "v"(xt[2]), "v"(xt[3]));
#pragma unroll
    for (int ps = 0; ps < 2; ++ps) {
      u32x4 g; g.x = xt[2 * ps]; g.y = tag; g.z = xt[2 * ps + 1]; g.w = tag;
      __builtin_amdgcn_raw_buffer_store_b128(g, rs, WS_XT + ((tid >> 2) + 128 * ps) * XTS + (8 * b + 2 * (tid & 3)) * 8, 0, 16);
    }
    f32x2 s = (v[0] + v[1]) + (v[2] + v[3]);
    const float mean = wave_sum(s.x + s.y) * (1.f / D);
    f32x2 mm; mm.x = mean; mm.y = mean;
    f32x2 s2 = {0.f, 0.f};
#pragma unroll
    for (int i = 0; i < 4; ++i) { f32x2 t = v[i] - mm; s2 = __builtin_elementwise_fma(t, t, s2); }
    const float var = wave_sum(s2.x + s2.y) * (1.f / D);
    const u32 mv = cvt2(var, mean);
    const float den = sqrtf(rbf(bf2f(mv & 0xffffu) + 1e-5f));
    const u32 pay = (mv & 0xffff0000u) | (cvt2(den, den) & 0xffffu);
    if (lane == 0)
      __hip_atomic_store((GAS u64*)(p.ws + WS_STATS) + (8 * b + w), ((u64)tag << 32) | (u64)pay, __ATOMIC_RELAXED, __HIP_MEMORY_SCOPE_AGENT);
  }
  STAMP(1);
  const int gmat = b >> 7, gks = (b >> 3) & 15, gpart = b & 7;
  const int gft0 = gpart < 6 ? 11 * gpart : 66 + 10 * (gpart - 6), gnft = gpart < 6 ? 11 : 10;
  if (w == 0) {
    const u16* base = (gmat ? p.wu : p.wg) + (32 * gks) * F;
#pragma unroll
    for (int j = 0; j < 11; ++j) {
      const int nn = 64 * j + lane;
      const int row = nn / 22, c = nn - row * 22;
      glds16<false>(base, (u32)(row * F + min(16 * gft0 + 8 * c, F - 8)) * 2u, lbase + LDS_STG + 8192 + j * 1024);
    }
  } else if (w == 1) {
#pragma unroll
    for (int un = 0; un < 2; ++un) {
      const int wunit = b + 256 * un;
      if (wunit < 344) {
        const int kap = wunit >> 3, dgp = wunit & 7;
        const int fa = kap < 28 ? 48 * kap : (kap < 42 ? 16 * (6 * (kap - 28) + 2) : 16 * 84);
        const int fb = kap < 28 ? 48 * kap + 16 : (kap < 42 ? 16 * (6 * (kap - 28) + 5) : 16 * 85);
#pragma unroll
        for (int it = 0; it < 4; ++it) {
          const int row = 8 * (it & 1) + (lane >> 3), c16 = lane & 7;
          const int doff = 16 * ((c16 >> 1) ^ ((row >> 1) & 3)) + 8 * (c16 & 1);
          glds16<false>(p.wd + (it >> 1 ? fb : fa) * D + 64 * dgp, (u32)(row * D + doff) * 2u, lbase + LDS_STG + un * 4096 + it * 1024);
        }
      }
    }
  }
  {
    const float dt = rbf(fast_expf(bf2f(v_ldt)));
    u32 m = max(max(cr2 & 0x7fffu, (cr2 >> 16) & 0x7fffu), max(ci2 & 0x7fffu, (ci2 >> 16) & 0x7fffu));
    m = wave_maxu(m);
    int ebound = ((int)(m >> 7) - 126) + ((int)((__float_as_uint(dt) >> 23) & 0xffu) - 126);
    ebound = max(-120, min(120, ebound));
    if ((w & 3) == 0 && lane == 0) *(float*)(smem + LDS_SC + cc * 4) = ldexpf(1.f, ebound - 6);
    const float a_r = -rbf(fast_expf(bf2f(v_lar))) * dt, a_i = bf2f(v_aim) * dt;
    const float ar2 = a_r * 1.44269504088896341f, airc = -a_i * 0.15915494309189535f;
    const int tb = LDS_TAB + cc * TABCH + n * 8;
    f32x2 Cc; Cc.x = ldexpf(bf2f(v_cr) * dt, 10 - ebound); Cc.y = -ldexpf(bf2f(v_ci) * dt, 10 - ebound);
#define PUTX(row, v) put_tab<false>(smem, tb + (row) * XROW, v)
#define PUTY(col, v) put_tab<true>(smem, tb + (41 + (col)) * XROW, v)
    if (part == 0) {
      f32x2 E0 = cexp_k(ar2, airc, 1.f), E1 = cmul(E0, E0), E2 = cmul(E1, E1), E7 = cexp_k(ar2, airc, 128.f), E8 = cmul(E7, E7);
      f32x2 T[8];
      T[0] = Cc; T[1] = cmul(E0, Cc); T[2] = cmul(E1, Cc); T[3] = cmul(E1, T[1]);
      T[4] = cmul(E2, Cc); T[5] = cmul(E2, T[1]); T[6] = cmul(E2, T[2]); T[7] = cmul(E2, T[3]);
#pragma unroll
      for (int e = 0; e < 8; ++e) { PUTX(e, T[e]); PUTX(8 + e, cmul(E7, T[e])); }
      f32x2 S[4];
      S[0] = cmul(E8, Cc); S[1] = cmul(E1, S[0]); S[2] = cmul(E2, S[0]); S[3] = cmul(E2, S[1]);
#pragma unroll
      for (int e = 0; e < 4; ++e) { PUTX(16 + e, S[e]); PUTX(20 + e, cmul(E7, S[e])); }
    } else {
      f32x2 E2 = cexp_k(ar2, airc, 4.f), E3 = cmul(E2, E2), E4 = cmul(E3, E3), E5 = cmul(E4, E4);
      f32x2 E6 = cexp_k(ar2, airc, 64.f), E7 = cmul(E6, E6), E8 = cmul(E7, E7), E9 = cmul(E8, E8);
      f32x2 E10 = cexp_k(ar2, airc, 1024.f), E11 = cmul(E10, E10);
      f32x2 Y[8];
      Y[0] = f32x2{1.f, 0.f}; Y[1] = E3; Y[2] = E4; Y[3] = cmul(E3, E4);
      Y[4] = E5; Y[5] = cmul(E5, E3); Y[6] = cmul(E5, E4); Y[7] = cmul(E5, Y[3]);
#pragma unroll
      for (int j = 0; j < 8; ++j) { PUTY(j, Y[j]); f32x2 t = (j == 0) ? E6 : cmul(E6, Y[j]); PUTY(8 + j, t); }
      f32x2 G[8];
      G[1] = E7; G[2] = E8; G[3] = cmul(E7, E8); G[4] = E9; G[5] = cmul(E9, E7); G[6] = cmul(E9, E8); G[7] = cmul(E9, G[3]);
      f32x2 V0 = cmul(E9, Cc), V1 = cmul(E2, V0);
      PUTX(24, V0); PUTX(25, V1);
#pragma unroll
      for (int i = 1; i < 4; ++i) { PUTX(24 + 2 * i, cmul(G[i], V0)); PUTX(25 + 2 * i, cmul(G[i], V1)); }
      f32x2 W0 = cmul(E10, Cc);
      PUTX(32, W0);
#pragma unroll
      for (int i = 1; i < 8; ++i) PUTX(32 + i, cmul(G[i], W0));
      PUTX(40, cmul(E11, Cc));
    }
  }
  if (w == 0) {
    WAIT_VM(0);
    const int ro = LDS_STG + 8192 + (4 * lg + tq) * 352 + tp * 8;
#pragma unroll
    for (int t = 0; t < 11; ++t) {
      if (t < gnft) {
        s16x4 a = tr_read(smem, ro + t * 32), c = tr_read(smem, ro + t * 32 + 16 * 352);
        const int ftile = gft0 + t, sl3 = ftile / 3, t6 = gmat * 3 + (ftile - 3 * sl3);
        __builtin_amdgcn_raw_buffer_store_b128(__builtin_bit_cast(u32x4, mkfrag(a, c)), rs, WS_WT + ((sl3 * 16 + gks) * 6 + t6) * 1024 + lane * 16, 0, 16);
      }
    }
  } else if (w == 1) {
    WAIT_VM(0);
    const int row = 4 * lg + tq;
#pragma unroll
    for (int un = 0; un < 2; ++un) {
      const int wunit = b + 256 * un;
      if (wunit < 344) {
        const int kap = wunit >> 3, dgp = wunit & 7;
#pragma unroll
        for (int dtl = 0; dtl < 4; ++dtl) {
          const int o = LDS_STG + un * 4096 + row * 128 + ((dtl ^ ((row >> 1) & 3)) * 32) + tp * 8;
          s16x4 a = tr_read(smem, o), c = tr_read(smem, o + 2048);
          __builtin_amdgcn_raw_buffer_store_b128(__builtin_bit_cast(u32x4, mkfrag(a, c)), rs, WS_WD + ((kap * 32 + dgp * 4 + dtl) * 64 + lane) * 16, 0, 16);
        }
      }
    }
  }
  STAMP(2);
  BARRIER();
  if (w < 6) {
    const int kc = w / 3, mt = w - 3 * kc;
    const int xa = LDS_TAB + kc * TABCH + (16 * mt + lr) * XROW + lg * 16;
    const int ya = LDS_TAB + kc * TABCH + (41 + lr) * XROW + lg * 16;
    f32x4 a0 = {0.f, 0.f, 0.f, 0.f}, a1 = {0.f, 0.f, 0.f, 0.f};
    u32x4 xa4[16], yb4[16];
#pragma unroll
    for (int ks = 0; ks < 16; ++ks) { xa4[ks] = *(const u32x4*)(smem + xa + ks * 64); yb4[ks] = *(const u32x4*)(smem + ya + ks * 64); }
#pragma unroll
    for (int ks = 0; ks < 16; ++ks) {
      u32x4 ys4; ys4.x = yb4[ks].y; ys4.y = yb4[ks].x; ys4.z = yb4[ks].w; ys4.w = yb4[ks].z;
      a0 = __builtin_amdgcn_mfma_f32_16x16x32_f16(__builtin_bit_cast(h16x8, xa4[ks]), __builtin_bit_cast(h16x8, yb4[ks]), a0, 0, 0, 0);
      a1 = __builtin_amdgcn_mfma_f32_16x16x32_f16(__builtin_bit_cast(h16x8, xa4[ks]), __builtin_bit_cast(h16x8, ys4), a1, 0, 0, 0);
    }
    char* kct = smem + LDS_KC + kc * KCCH;
    if (mt == 0 && lane == 0) *(_Float16*)(kct + 1441 * 2) = (_Float16)0.f;
#pragma unroll
    for (int r = 0; r < 4; ++r) {
      const int row = 16 * mt + 4 * lg + r, j = lr;
      int pos;
      if (row < 16) pos = (row & 7) + 128 * (row >> 3) + 8 * j;
      else if (row < 24) pos = 256 + 2 * (row & 3) + 128 * ((row - 16) >> 2) + 8 * j;
      else if (row < 32) pos = 512 + 4 * (row & 1) + 128 * ((row - 24) >> 1) + 8 * j;
      else pos = 1024 + 128 * (row - 32) + 8 * j;
      const int code = pos == 0 ? 1440 : (int)(__float_as_uint((float)pos) >> 16) - 0x3F80;
      if (row < 40 || (row == 40 && j == 0)) *(_Float16*)(kct + code * 2) = (_Float16)((a0[r] + a1[r]) * 0.125f);
    }
  }
  STAMP(3);
  BARRIER();
  if (w < 2) {
    WAIT_VM(0);
    if (lds_inc(smem, LDS_CNT + 28, lane) == 1) publish8(p.ws, WS_FLAGW, b, tag, lane);
  }
  const u32 xto = WS_XT + (u32)(ch0 >> 1) * XTS + tid * 32, sto = WS_STATS + tid * 32;
  u32x4 gx0 = __builtin_amdgcn_raw_buffer_load_b128(rs, xto, 0, 16), gx1 = __builtin_amdgcn_raw_buffer_load_b128(rs, xto + 16, 0, 16);
  u32x4 gs0 = __builtin_amdgcn_raw_buffer_load_b128(rs, sto, 0, 16), gs1 = __builtin_amdgcn_raw_buffer_load_b128(rs, sto + 16, 0, 16);
#pragma unroll
  for (int rnd = 0; rnd < 3; ++rnd) {
    const int task = tid + rnd * NT;
    if (task < 1032) {
      const int kc = task >= 516, m = task - kc * 516;
      const char* kct = smem + LDS_KC + kc * KCCH - 0x3F80 * 2;
      u32 r[7];
      if (m < 511) {
#pragma unroll
        for (int e = 0; e < 7; ++e) {
          const u32 fb = __float_as_uint((float)(2048 - 4 * m - e));
          r[e] = *(const u16*)(kct + (((fb + 0x7FFFu + ((fb >> 16) & 1u)) >> 16) << 1));
        }
      } else {
#pragma unroll
        for (int e = 0; e < 7; ++e) {
          const int idx = 2048 - 4 * m - e;
          const u32 fb = __float_as_uint((float)max(idx, 1));
          const u32 code = idx > 0 ? (fb >> 16) : (idx == 0 ? 1440u + 0x3F80u : 1441u + 0x3F80u);
          r[e] = *(const u16*)(kct + (code << 1));
        }
      }
      const int fo = LDS_F + kc * 4 * FCOPY + m * 8;
#pragma unroll
      for (int s = 0; s < 4; ++s) {
        u32x2 v; v.x = r[s] | (r[s + 1] << 16); v.y = r[s + 2] | (r[s + 3] << 16);
        *(u32x2*)(smem + fo + s * FCOPY) = v;
      }
    }
  }
  STAMP(4);
  u32 unb[4], xcol[4];
  {
    for (u32 spins = 0; spins < SPIN_CAP; ++spins) {
      const bool ok = gx0.y == tag && gx0.w == tag && gx1.y == tag && gx1.w == tag && gs0.y == tag && gs0.w == tag && gs1.y == tag && gs1.w == tag;
      if (__all(ok)) break;
      __builtin_amdgcn_s_sleep(2);
      asm volatile("" ::: "memory");
      if (!ok) {
        gx0 = __builtin_amdgcn_raw_buffer_load_b128(rs, xto, 0, 16); gx1 = __builtin_amdgcn_raw_buffer_load_b128(rs, xto + 16, 0, 16);
        gs0 = __builtin_amdgcn_raw_buffer_load_b128(rs, sto, 0, 16); gs1 = __builtin_amdgcn_raw_buffer_load_b128(rs, sto + 16, 0, 16);
      }
    }
    asm volatile("" ::: "memory");
    xcol[0] = gx0.x; xcol[1] = gx0.z; xcol[2] = gx1.x; xcol[3] = gx1.z;
    const u32 st[4] = {gs0.x, gs0.z, gs1.x, gs1.z};
    u32 uh0[2], uh1[2];
#pragma unroll
    for (int i = 0; i < 4; ++i) {
      const float m = bfhi(st[i]), den = bf2f(st[i] & 0xffffu);
      const float rc = __builtin_amdgcn_rcpf(den);
      const u32 ab = cvt2(bf2f(xcol[i] & 0xffffu) - m, bfhi(xcol[i]) - m);
      f32x2 a; a.x = bf2f(ab & 0xffffu); a.y = bfhi(ab);
      f32x2 rc2 = {rc, rc}, nden = {-den, -den};
      f32x2 qv = a * rc2;
      qv = __builtin_elementwise_fma(__builtin_elementwise_fma(qv, nden, a), rc2, qv);
      unb[i] = cvt2(qv.x, qv.y);
      const u32 h0 = __builtin_bit_cast(u16, (_Float16)bf2f(unb[i] & 0xffffu)), h1 = __builtin_bit_cast(u16, (_Float16)bfhi(unb[i]));
      if (i & 1) { uh0[i >> 1] |= h0 << 16; uh1[i >> 1] |= h1 << 16; } else { uh0[i >> 1] = h0; uh1[i >> 1] = h1; }
    }
    const int uo = LDS_U + (240 + 4 * tid) * 2;
    u32x2 v0; v0.x = uh0[0]; v0.y = uh0[1];
    u32x2 v1; v1.x = uh1[0]; v1.y = uh1[1];
    *(u32x2*)(smem + uo) = v0;
    *(u32x2*)(smem + uo + UCH) = v1;
    if (tid < 120) { *(u32*)(smem + LDS_U + tid * 4) = 0u; *(u32*)(smem + LDS_U + UCH + tid * 4) = 0u; }
  }
  STAMP(5);
  BARRIER();
  if (b == 0 && tid == 0)
    __hip_atomic_store((GAS u32*)(p.ws + WS_GEN), tag, __ATOMIC_RELAXED, __HIP_MEMORY_SCOPE_AGENT);

  const int rot = (sl * 5 + pi * 8) & 15;
  const float sc0 = *(const float*)(smem + LDS_SC), sc1 = *(const float*)(smem + LDS_SC + 4);
  auto epilogue = [&]() {
    const int l0 = 4 * (tid & 255);
    const int c = l0 >> 8, i = (l0 >> 4) & 15, g = tid & 3;
    const int ro = LDS_PART + c * 1024 + (16 * g + ((i + 4 * g) & 15)) * 16;
    const float dk0 = bf2f(dsk2 & 0xffffu), dk1 = bfhi(dsk2);
    u32 hw[2][2];
#pragma unroll
    for (int k2 = 0; k2 < 2; ++k2) {
      f32x4 y = (*(const f32x4*)(smem + ro + (2 * k2) * 4096) + *(const f32x4*)(smem + ro + (2 * k2 + 1) * 4096)) * (k2 ? sc1 : sc0);
      const float dk = k2 ? dk1 : dk0;
#pragma unroll
      for (int pr = 0; pr < 2; ++pr) {
        const u32 u0 = unb[2 * pr], u1 = unb[2 * pr + 1], x0 = xcol[2 * pr], x1 = xcol[2 * pr + 1];
        const u32 yb = cvt2(y[2 * pr], y[2 * pr + 1]);
        const float un0 = k2 ? bfhi(u0) : bf2f(u0 & 0xffffu), un1 = k2 ? bfhi(u1) : bf2f(u1 & 0xffffu);
        const u32 sk = cvt2(un0 * dk, un1 * dk);
        const u32 ab = cvt2(bf2f(yb & 0xffffu) + bf2f(sk & 0xffffu), bfhi(yb) + bfhi(sk));
        const float xv0 = k2 ? bfhi(x0) : bf2f(x0 & 0xffffu), xv1 = k2 ? bfhi(x1) : bf2f(x1 & 0xffffu);
        hw[k2][pr] = cvt2(xv0 + bf2f(ab & 0xffffu), xv1 + bfhi(ab));
      }
    }
    const bool odd = tid & 1;
    const u32 s0 = odd ? hw[0][0] : hw[1][0], s1 = odd ? hw[0][1] : hw[1][1];
    const u32 r0 = (u32)__builtin_amdgcn_update_dpp(0, (int)s0, 0xB1, 0xf, 0xf, true), r1 = (u32)__builtin_amdgcn_update_dpp(0, (int)s1, 0xB1, 0xf, 0xf, true);
    u32x4 o;
    if (odd) { o.x = r0; o.y = r1; o.z = hw[1][0]; o.w = hw[1][1]; }
    else { o.x = hw[0][0]; o.y = hw[0][1]; o.z = r0; o.w = r1; }
    const int dd = ch0 + (odd ? 1 : 0), ll = 4 * (tid & ~1);
    __builtin_amdgcn_raw_buffer_store_b128(o, rs, WS_HT + ((ll >> 5) * D + dd) * 64 + (ll & 31) * 2, 0, 16);
  };

  if (isQ) {
    const int qw = w - 4, kc = qw >> 1, rh = qw & 1;
    const int sft = (4 - (lr & 3)) & 3;
    const int fbase = LDS_F + kc * 4 * FCOPY + sft * FCOPY + 2 * (2032 + 8 * lg - (lr + sft)) - 512 * 7;
    const int gbase = LDS_U + kc * UCH + 2 * (224 + 16 * lr + 8 * lg);
    const int po = LDS_PART + qw * 4096 + (16 * lg + ((lr + 4 * lg) & 15)) * 16;
    f32x4 acc[4];
    conv_part<0, 4>(smem, fbase, gbase, rh, acc);
#pragma unroll
    for (int c = 0; c < 4; ++c) *(f32x4*)(smem + po + c * 1024) = acc[c];
    lds_inc(smem, LDS_CNT + 32, lane);
    STAMP(6);
    conv_part<4, 8>(smem, fbase, gbase, rh, acc);
    STAMP(7);
    lds_inc(smem, LDS_CNT + 36, lane);
    lds_wait(smem, LDS_CNT + 8, 4);
#pragma unroll
    for (int c = 0; c < 4; ++c) *(f32x4*)(smem + po + c * 1024) = acc[c];
    lds_inc(smem, LDS_CNT + 40, lane);
    lds_wait(smem, LDS_CNT + 40, 4);
    epilogue();
    WAIT_VM(0);
    STAMP(8);
    if (lds_inc(smem, LDS_CNT + 0, lane) == 3) publish8(p.ws, WS_FLAGB + 8192, b, tag, lane);
    lds_wait(smem, LDS_CNT + 0, 4);
    if (c_active) lds_wait(smem, LDS_CNT + 4, 4);
  } else {
    if (c_active) {
      if (w == 0) { wait_flags256(rs, WS_FLAGW + xcd * 1024, tag, lane); lds_inc(smem, LDS_CNT + 44, lane); }
    }
    STAMP(6);
    lds_wait(smem, LDS_CNT + 32, 4);
    epilogue();
    lds_inc(smem, LDS_CNT + 8, lane);
    if (c_active) {
      lds_wait(smem, LDS_CNT + 44, 1);
      const char* wsrc = p.ws + WS_WT + slice * 98304 + w * 24576;
#pragma unroll
      for (int jj = 0; jj < 24; ++jj) glds16<true>(wsrc + jj * 1024, (u32)lane * 16u, lbase + LDS_W + w * 24576 + jj * 1024);
    }
    WAIT_VM(0);
    STAMP(7);
    if (lds_inc(smem, LDS_CNT + 48, lane) == 3) publish8(p.ws, WS_FLAGB, b, tag, lane);
    if (c_active) {
      lds_wait(smem, LDS_CNT + 48, 4);
      for (int tl = w; tl < 6; tl += 4) {
        u32x4 one4 = {0x3F803F80u, 0x3F803F80u, 0x3F803F80u, 0x3F803F80u};
        f32x4 a = {0.f, 0.f, 0.f, 0.f};
#pragma unroll
        for (int ks = 0; ks < 16; ++ks) {
          const b16x8 wf = *(const b16x8*)(smem + LDS_W + (ks * 6 + tl) * 1024 + lane * 16);
          a = __builtin_amdgcn_mfma_f32_16x16x32_bf16(wf, __builtin_bit_cast(b16x8, one4), a, 0, 0, 0);
        }
        if (lr == 0) *(f32x4*)(smem + LDS_CS + (tl * 16 + 4 * lg) * 4) = a;
      }
      lds_inc(smem, LDS_CNT + 4, lane);
      lds_wait(smem, LDS_CNT + 36, 4);
    }
  }

  if (c_active) {
    const int grp = w >> 2, gw = w & 3;
    const int l0 = 1024 * grp + 256 * pg + 128 * pi + 32 * gw;
    const int ring = grp ? (LDS_RINGQ + gw * 7168) : (LDS_RINGP + gw * 8192);
    if (gw == 0) { wait_flags256(rs, WS_FLAGB + grp * 8192 + xcd * 1024, tag, lane); lds_inc(smem, LDS_CNT + 12 + grp * 4, lane); }
    else lds_wait(smem, LDS_CNT + 12 + grp * 4, 1);
    STAMP(9);
    const int drow = lane >> 2, dc16 = lane & 3;
    const u32 hvoff = (u32)(drow * 64 + ((dc16 >> 1) ^ ((drow >> 2) & 1)) * 32 + (dc16 & 1) * 16);
    const char* hbase = p.ws + WS_HT + (l0 >> 5) * 32768;
#define HDMA(hs) glds16<true>(hbase + (((((hs) >> 1) + rot) & 15) * 2048 + ((hs) & 1) * 1024), hvoff, lbase + ring + ((hs) % 7) * 1024)
#pragma unroll
    for (int hs = 0; hs < 7; ++hs) HDMA(hs);
    const int hlane = ring + (4 * lg + tq) * 64 + tp * 8;
    const int hx0 = ((0 ^ (lg & 1)) * 32), hx1 = ((1 ^ (lg & 1)) * 32);
    f32x4 ag[3][2], au[3][2];
#pragma unroll
    for (int ft = 0; ft < 3; ++ft)
#pragma unroll
      for (int lt = 0; lt < 2; ++lt) { ag[ft][lt] = f32x4{0.f, 0.f, 0.f, 0.f}; au[ft][lt] = f32x4{0.f, 0.f, 0.f, 0.f}; }
    float s1[2] = {0.f, 0.f}, s2[2] = {0.f, 0.f};
    const b16x2 ones = __builtin_bit_cast(b16x2, 0x3F803F80u);
    b16x8 wgf[2][3], wuf[2][3], hf[2][2];
    auto rdw = [&](int i, int buf) {
      const int wa = LDS_W + lane * 16 + ((i + rot) & 15) * 6144;
#pragma unroll
      for (int ft = 0; ft < 3; ++ft) {
        wgf[buf][ft] = *(const b16x8*)(smem + wa + ft * 1024);
        wuf[buf][ft] = *(const b16x8*)(smem + wa + (3 + ft) * 1024);
      }
    };
    auto rdh = [&](int i, int buf) {
      const int sa = hlane + ((2 * i) % 7) * 1024, sb = hlane + ((2 * i + 1) % 7) * 1024;
      hf[buf][0] = mkfrag(tr_read(smem, sa + hx0), tr_read(smem, sb + hx0));
      hf[buf][1] = mkfrag(tr_read(smem, sa + hx1), tr_read(smem, sb + hx1));
    };
    rdw(0, 0);
    WAIT_VM(5);
    rdh(0, 0);
#pragma unroll
    for (int i = 0; i < 16; ++i) {
      const int cur = i & 1;
      asm volatile("" :: "v"(hf[cur][0]), "v"(hf[cur][1]));
      if (2 * i + 7 < 32) HDMA(2 * i + 7);
      if (2 * i + 8 < 32) HDMA(2 * i + 8);
      if (i < 15) {
        rdw(i + 1, cur ^ 1);
        if (i <= 11) WAIT_VM(5); else if (i == 12) WAIT_VM(4); else if (i == 13) WAIT_VM(2); else WAIT_VM(0);
        rdh(i + 1, cur ^ 1);
      }
#pragma unroll
      for (int lt = 0; lt < 2; ++lt) {
        u32x4 hv = __builtin_bit_cast(u32x4, hf[cur][lt]);
#pragma unroll
        for (int e = 0; e < 4; ++e) {
          const u32 hve = hv[e];
          b16x2 pr = __builtin_bit_cast(b16x2, hve);
          s1[lt] = __builtin_amdgcn_fdot2_f32_bf16(pr, ones, s1[lt], false);
          s2[lt] = __builtin_amdgcn_fdot2_f32_bf16(pr, pr, s2[lt], false);
        }
      }
#pragma unroll
      for (int ft = 0; ft < 3; ++ft)
#pragma unroll
        for (int lt = 0; lt < 2; ++lt) {
          ag[ft][lt] = __builtin_amdgcn_mfma_f32_16x16x32_bf16(wgf[cur][ft], hf[cur][lt], ag[ft][lt], 0, 0, 0);
          au[ft][lt] = __builtin_amdgcn_mfma_f32_16x16x32_bf16(wuf[cur][ft], hf[cur][lt], au[ft][lt], 0, 0, 0);
        }
    }
    STAMP(10);
    if (!isQ) lds_wait(smem, LDS_CNT + 4, 4);
    u32 hp[3][2][2];
#pragma unroll
    for (int lt = 0; lt < 2; ++lt) {
      const float mean = sum_rows(s1[lt]) * (1.f / D);
      const float var = fmaxf(sum_rows(s2[lt]) * (1.f / D) - mean * mean, 0.f);
      const u32 mv = cvt2(var, mean);
      const float m = bfhi(mv);
      const float den = rbf(sqrtf(rbf(bf2f(mv & 0xffffu) + 1e-5f)));
      const float rinv = 1.0f / den;
      const f32x2 nm2 = {-m, -m}, ri2 = {rinv, rinv};
#pragma unroll
      for (int ft = 0; ft < 3; ++ft) {
        const f32x4 cg = *(const f32x4*)(smem + LDS_CS + (ft * 16 + 4 * lg) * 4), cu = *(const f32x4*)(smem + LDS_CS + (48 + ft * 16 + 4 * lg) * 4);
#pragma unroll
        for (int pr = 0; pr < 2; ++pr) {
          f32x2 g2 = {ag[ft][lt][2 * pr], ag[ft][lt][2 * pr + 1]}, u2 = {au[ft][lt][2 * pr], au[ft][lt][2 * pr + 1]};
          f32x2 cg2 = {cg[2 * pr], cg[2 * pr + 1]}, cu2 = {cu[2 * pr], cu[2 * pr + 1]};
          g2 = __builtin_elementwise_fma(nm2, cg2, g2) * ri2;
          u2 = __builtin_elementwise_fma(nm2, cu2, u2) * ri2;
          const u32 gb = cvt2(g2.x, g2.y), ub = cvt2(u2.x, u2.y);
          f32x2 gr = {fmaxf(bf2f(gb & 0xffffu), 0.f), fmaxf(bfhi(gb), 0.f)}, ur = {bf2f(ub & 0xffffu), bfhi(ub)};
          gr = gr * ur;
          hp[ft][lt][pr] = cvt2(gr.x, gr.y);
        }
      }
    }
    const int lt0 = l0 >> 4;
    if (slice < 28) {
#pragma unroll
      for (int lt = 0; lt < 2; ++lt) {
        u32x4 v; v.x = hp[0][lt][0]; v.y = hp[0][lt][1]; v.z = hp[1][lt][0]; v.w = hp[1][lt][1];
        __builtin_amdgcn_raw_buffer_store_b128(v, rs, WS_PB + ((slice * 128 + lt0 + lt) * 64 + lane) * 16, 0, 16);
      }
      u32x4 v; v.x = hp[2][0][0]; v.y = hp[2][0][1]; v.z = hp[2][1][0]; v.w = hp[2][1][1];
      __builtin_amdgcn_raw_buffer_store_b128(v, rs, WS_SB + ((slice * 64 + (lt0 >> 1)) * 64 + lane) * 16, 0, 16);
    } else {
#pragma unroll
      for (int ft = 0; ft < 2; ++ft) {
        u32x4 v; v.x = hp[ft][0][0]; v.y = hp[ft][0][1]; v.z = hp[ft][1][0]; v.w = hp[ft][1][1];
        __builtin_amdgcn_raw_buffer_store_b128(v, rs, WS_SB + (((28 + ft) * 64 + (lt0 >> 1)) * 64 + lane) * 16, 0, 16);
      }
    }
    WAIT_VM(0);
    if (lane == 0)
      __hip_atomic_store((GAS u32*)(p.ws + WS_FLAGH) + (grp * 32 + ((l0 >> 5) & 31)) * 32 + slice, tag, __ATOMIC_RELAXED, __HIP_MEMORY_SCOPE_AGENT);
    STAMP(11);
  }

  {
    const int rg = slot >> 3, dg = slot & 7;
    const int pair0 = isQ ? 16 + 3 * (w - 4) : 4 * w;
    const int sing0 = isQ ? 4 + 3 * (w - 4) : w;
    wait_flags256(rs, WS_FLAGW + xcd * 1024, tag, lane);
    u32x4 wd[6][4];
#pragma unroll
    for (int i = 0; i < 6; ++i) {
      int kap;
      if (isQ) kap = i < 3 ? pair0 + i : min(28 + sing0 + (i - 3), 42);
      else kap = i < 4 ? pair0 + i : 28 + sing0;
#pragma unroll
      for (int dtl = 0; dtl < 4; ++dtl)
        if (isQ || i < 5) wd[i][dtl] = __builtin_amdgcn_raw_buffer_load_b128(rs, WS_WD + ((kap * 32 + dg * 4 + dtl) * 64 + lane) * 16, 0, 16);
    }
    STAMP(12);
    const int ll = tid >> 4, dc = tid & 15;
#pragma unroll
    for (int st = 0; st < 2; ++st) {
      const int l0 = 1024 * st + 128 * xcd + 32 * rg;
      const int lt0 = l0 >> 4;
      if (w == 0) {
        wait_flags256(rs, WS_FLAGB + st * 8192 + xcd * 1024, tag, lane);
        const u32 fo = WS_FLAGH + ((st * 32 + 4 * xcd + rg) * 32) * 4;
        for (u32 spins = 0; spins < SPIN_CAP; ++spins) {
          asm volatile("" ::: "memory");
          u32 v = __builtin_amdgcn_raw_buffer_load_b32(rs, fo + (lane & 31) * 4, 0, 16);
          bool ok = v == tag || (lane & 31) >= 29;
          if (__all(ok)) break;
          __builtin_amdgcn_s_sleep(2);
        }
        lds_inc(smem, LDS_CNT + 20 + st * 4, lane);
      } else lds_wait(smem, LDS_CNT + 20 + st * 4, 1);
      STAMP(13 + 4 * st);
      if (st == 0) BARRIER();
      if (w < 4)
        glds16<true>(p.ws + WS_HT + (l0 >> 5) * 32768 + (64 * dg) * 64 + w * 1024, (u32)lane * 16u, lbase + LDS_RES + st * 4096 + w * 1024);
      u32x4 ha[6], hb[6];
      const u32 pbase = WS_PB + ((pair0 * 128 + lt0) * 64 + lane) * 16;
      const u32 sbase = WS_SB + ((2 * sing0 * 64 + (lt0 >> 1)) * 64 + lane) * 16;
#pragma unroll
      for (int i = 0; i < 6; ++i) {
        const bool is_pair = isQ ? i < 3 : i < 4;
        const int si = isQ ? i - 3 : i - 4;
        if (isQ || i < 5) {
          u32 oa, ob;
          if (is_pair) { oa = pbase + i * 131072; ob = oa + 1024; }
          else { oa = sbase + ((w == 7 && si == 2) ? 1 : si) * 131072; ob = oa + 65536; }
          ha[i] = __builtin_amdgcn_raw_buffer_load_b128(rs, oa, 0, 16);
          hb[i] = __builtin_amdgcn_raw_buffer_load_b128(rs, ob, 0, 16);
        }
      }
      f32x4 acc[4][2];
#pragma unroll
      for (int dtl = 0; dtl < 4; ++dtl) { acc[dtl][0] = f32x4{0.f, 0.f, 0.f, 0.f}; acc[dtl][1] = f32x4{0.f, 0.f, 0.f, 0.f}; }
#pragma unroll
      for (int i = 0; i < 6; ++i) {
        const bool is_pair = isQ ? i < 3 : i < 4;
        if (isQ || i < 5) {
          u32x4 f0, f1;
          if (is_pair) { f0 = ha[i]; f1 = hb[i]; }
          else {
            f0.x = ha[i].x; f0.y = ha[i].y; f0.z = hb[i].x; f0.w = hb[i].y;
            f1.x = ha[i].z; f1.y = ha[i].w; f1.z = hb[i].z; f1.w = hb[i].w;
          }
          if (i == 5 && w == 7) { f0 = u32x4{0u, 0u, 0u, 0u}; f1 = f0; }
#pragma unroll
          for (int dtl = 0; dtl < 4; ++dtl) {
            acc[dtl][0] = __builtin_amdgcn_mfma_f32_16x16x32_bf16(__builtin_bit_cast(b16x8, wd[i][dtl]), __builtin_bit_cast(b16x8, f0), acc[dtl][0], 0, 0, 0);
            acc[dtl][1] = __builtin_amdgcn_mfma_f32_16x16x32_bf16(__builtin_bit_cast(b16x8, wd[i][dtl]), __builtin_bit_cast(b16x8, f1), acc[dtl][1], 0, 0, 0);
          }
        }
      }
      STAMP(14 + 4 * st);
#pragma unroll
      for (int dtl = 0; dtl < 4; ++dtl)
#pragma unroll
        for (int lt = 0; lt < 2; ++lt)
          *(f32x4*)(smem + LDS_P2 + st * 65536 + ((w * 4 + dtl) * 2 + lt) * 1024 + lane * 16) = acc[dtl][lt];
      WAIT_VM(0);
      BARRIER();
      STAMP(15 + 4 * st);
      {
        const int dtl = dc >> 2, g = dc & 3, lt = ll >> 4;
        f32x4 s = {0.f, 0.f, 0.f, 0.f};
#pragma unroll
        for (int k8 = 0; k8 < 8; ++k8)
          s += *(const f32x4*)(smem + LDS_P2 + st * 65536 + ((k8 * 4 + dtl) * 2 + lt) * 1024 + (g * 16 + (ll & 15)) * 16);
        const u16* rp = (const u16*)(smem + LDS_RES + st * 4096 + (4 * dc) * 64 + ll * 2);
        const u32 a0 = cvt2(s[0], s[1]), a1 = cvt2(s[2], s[3]);
        u32x2 o;
        o.x = cvt2(bf2f(rp[0]) + bf2f(a0 & 0xffffu), bf2f(rp[32]) + bfhi(a0));
        o.y = cvt2(bf2f(rp[64]) + bf2f(a1 & 0xffffu), bf2f(rp[96]) + bfhi(a1));
        *(u32x2*)(p.out + (l0 + ll) * D + 64 * dg + 4 * dc) = o;
      }
      STAMP(16 + 4 * st);
    }
  }
}

extern "C" void kernel_launch(void* const* d_in, const int* in_sizes, int n_in, void* d_out, int out_size, void* d_ws,
                              size_t ws_size, hipStream_t stream) {
  Params p;
  p.x = (const u16*)d_in[0]; p.lar = (const u16*)d_in[1]; p.aim = (const u16*)d_in[2]; p.cr = (const u16*)d_in[3];
  p.ci = (const u16*)d_in[4]; p.ldt = (const u16*)d_in[5]; p.dsk = (const u16*)d_in[6];
  p.wg = (const u16*)d_in[8]; p.wu = (const u16*)d_in[9]; p.wd = (const u16*)d_in[10];
  p.out = (u16*)d_out; p.ws = (char*)d_ws;
  (void)hipFuncSetAttribute((const void*)mega, hipFuncAttributeMaxDynamicSharedMemorySize, LDS_TOTAL);
  mega<<<NWG, NT, LDS_TOTAL, stream>>>(p);
}
```

```cpp
#include <hip/hip_runtime.h>
#include <hip/hip_bf16.h>
#include <stdint.h>

typedef unsigned short u16;
typedef unsigned int u32;
typedef unsigned long long u64;
typedef _Float16 h16x8 __attribute__((ext_vector_type(8)));
typedef _Float16 h16x2 __attribute__((ext_vector_type(2)));
typedef __bf16 b16x8 __attribute__((ext_vector_type(8)));
typedef __bf16 b16x2 __attribute__((ext_vector_type(2)));
typedef short s16x4 __attribute__((ext_vector_type(4)));
typedef float f32x4 __attribute__((ext_vector_type(4)));
typedef float f32x2 __attribute__((ext_vector_type(2)));
typedef u32 u32x4 __attribute__((ext_vector_type(4)));
typedef u32 u32x2 __attribute__((ext_vector_type(2)));
#define LAS __attribute__((address_space(3)))
#define GAS __attribute__((address_space(1)))

static constexpr int L = 2048, D = 512, NM = 128, F = 1376;
static constexpr int NWG = 256, NT = 512;

static constexpr u32 WS_GEN = 0;
static constexpr u32 WS_STATS = 16384;
static constexpr u32 WS_STAT2 = 32768;
static constexpr u32 FSET = 8 * 64 * 128;
static constexpr u32 WS_FLAGB = 65536;
static constexpr u32 WS_FLAGW = WS_FLAGB + 2 * FSET;
static constexpr u32 WS_FLAGW2 = WS_FLAGW + FSET;
static constexpr u32 WS_FLAGH = WS_FLAGW2 + FSET;
static constexpr u32 WS_HT = WS_FLAGH + 64 * 1024;
static constexpr u32 WS_PB = WS_HT + D * L * 2;
static constexpr u32 WS_SB = WS_PB + 28 * 128 * 1024;
static constexpr u32 WS_WD = WS_SB + 30 * 64 * 1024;
static constexpr u32 XTS = 16384 + 128;
static constexpr u32 WS_XT = WS_WD + 43 * 32 * 1024;
static constexpr u32 WS_WT = WS_XT + 256 * XTS;
static constexpr u32 WS_END = WS_WT + 29 * 98304;

static constexpr int XROW = 1056;
static constexpr int LDS_TAB = 0;
static constexpr int TABCH = 57 * XROW;
static constexpr int LDS_W = 0;
static constexpr int LDS_F = 98304;
static constexpr int FCOPY = 4160;
static constexpr int LDS_PART = 131584;
static constexpr int LDS_STG = 120832;
static constexpr int LDS_KC = 141824;
static constexpr int KCCH = 2944;
static constexpr int LDS_U = 150784;
static constexpr int UCH = 2288 * 2;
static constexpr int LDS_CS = 160000;
static constexpr int LDS_SC = 160384;
static constexpr int LDS_CNT = 160400;
static constexpr int LDS_RINGP = 98304;
static constexpr int LDS_RINGQ = 131072;
static constexpr int LDS_P2 = 0;
static constexpr int LDS_RES = 131072;
static constexpr int LDS_JUNK = 163584;
static constexpr int LDS_TOTAL = 163840;

struct Params {
  const u16* x; const u16* lar; const u16* aim; const u16* cr; const u16* ci; const u16* ldt; const u16* dsk;
  const u16* wg; const u16* wu; const u16* wd; u16* out; char* ws;
};

__device__ __forceinline__ float bf2f(u32 v) { return __uint_as_float(v << 16); }
__device__ __forceinline__ float bfhi(u32 v) { return __uint_as_float(v & 0xffff0000u); }
__device__ __forceinline__ u32 cvt2(float lo, float hi) {
  f32x2 v; v.x = lo; v.y = hi;
  b16x2 r = __builtin_convertvector(v, b16x2);
  return __builtin_bit_cast(u32, r);
}
__device__ __forceinline__ float rbf(float f) { return bf2f(cvt2(f, f) & 0xffffu); }

__device__ __forceinline__ f32x2 cmul(f32x2 b, f32x2 a) {
  f32x2 t = a.xx * b;
  f32x2 bs; bs.x = -b.y; bs.y = b.x;
  return __builtin_elementwise_fma(a.yy, bs, t);
}
__device__ __forceinline__ f32x2 cexp_k(float ar2, float airev, float sc) {
  const float e = __builtin_amdgcn_exp2f(ar2 * sc);
  const float rev = __builtin_amdgcn_fractf(airev * sc);
  f32x2 o; o.x = __builtin_amdgcn_cosf(rev); o.y = __builtin_amdgcn_sinf(rev);
  return o * e;
}
__device__ __forceinline__ float fast_expf(float x) {
  const float t = x * 1.44269504088896341f;
  const float r = __builtin_fmaf(x, 1.44269504088896341f, -t) + x * 1.925963033500e-08f;
  return __builtin_amdgcn_exp2f(t) * __builtin_fmaf(r, 0.6931471805599453f, 1.0f);
}

#define BARRIER() do { asm volatile("s_waitcnt lgkmcnt(0)" ::: "memory"); __builtin_amdgcn_s_barrier(); asm volatile("" ::: "memory"); } while (0)
#define WAIT_VM(n) asm volatile("s_waitcnt vmcnt(" #n ")" ::: "memory")
#define SCHED() __builtin_amdgcn_sched_barrier(0)
#define WAIT_LGKM0() __builtin_amdgcn_s_waitcnt(0xc07f)
static constexpr u32 SPIN_CAP = 1u << 17;

__device__ __forceinline__ __amdgpu_buffer_rsrc_t mk_rsrc(const void* p, u32 bytes) {
  return __builtin_amdgcn_make_buffer_rsrc((void*)p, 0, bytes, 0x00020000);
}
template <int CTRL, int RM>
__device__ __forceinline__ float dpp_addf(float v) {
  return v + __int_as_float(__builtin_amdgcn_update_dpp(0, __float_as_int(v), CTRL, RM, 0xf, true));
}
__device__ __forceinline__ float wave_sum(float v) {
  v = dpp_addf<0x111, 0xf>(v); v = dpp_addf<0x112, 0xf>(v); v = dpp_addf<0x114, 0xf>(v); v = dpp_addf<0x118, 0xf>(v);
  v = dpp_addf<0x142, 0xa>(v); v = dpp_addf<0x143, 0xc>(v);
  return __int_as_float(__builtin_amdgcn_readlane(__float_as_int(v), 63));
}
template <int CTRL, int RM>
__device__ __forceinline__ u32 dpp_maxu(u32 v) {
  u32 o = (u32)__builtin_amdgcn_update_dpp(0, (int)v, CTRL, RM, 0xf, true);
  return v > o ? v : o;
}
__device__ __forceinline__ u32 wave_maxu(u32 v) {
  v = dpp_maxu<0x111, 0xf>(v); v = dpp_maxu<0x112, 0xf>(v); v = dpp_maxu<0x114, 0xf>(v); v = dpp_maxu<0x118, 0xf>(v);
  v = dpp_maxu<0x142, 0xa>(v); v = dpp_maxu<0x143, 0xc>(v);
  return (u32)__builtin_amdgcn_readlane((int)v, 63);
}
__device__ __forceinline__ float sum_rows(float v) {
  auto a = __builtin_amdgcn_permlane16_swap(__float_as_uint(v), __float_as_uint(v), false, false);
  v = __uint_as_float(a[0]) + __uint_as_float(a[1]);
  auto c = __builtin_amdgcn_permlane32_swap(__float_as_uint(v), __float_as_uint(v), false, false);
  return __uint_as_float(c[0]) + __uint_as_float(c[1]);
}
template <bool NEGIM>
__device__ __forceinline__ void put_tab(char* smem, int off, f32x2 v) {
  u32 h, l;
  const float re = v.x, im = v.y;
  if constexpr (NEGIM)
    asm("v_cvt_pkrtz_f16_f32 %0, %2, -%3\n\tv_fma_mixlo_f16 %1, -%0, 1.0, %2 op_sel_hi:[1,0,0]\n\t"
        "v_fma_mixhi_f16 %1, -%0, 1.0, -%3 op_sel:[1,0,0] op_sel_hi:[1,0,0]" : "=&v"(h), "=&v"(l) : "v"(re), "v"(im));
  else
    asm("v_cvt_pkrtz_f16_f32 %0, %2, %3\n\tv_fma_mixlo_f16 %1, -%0, 1.0, %2 op_sel_hi:[1,0,0]\n\t"
        "v_fma_mixhi_f16 %1, -%0, 1.0, %3 op_sel:[1,0,0] op_sel_hi:[1,0,0]" : "=&v"(h), "=&v"(l) : "v"(re), "v"(im));
  u32x2 o; o.x = h; o.y = l;
  *(u32x2*)(smem + off) = o;
}
__device__ __forceinline__ s16x4 tr_read(char* smem, int off) {
  return __builtin_amdgcn_ds_read_tr16_b64_v4i16((LAS s16x4*)(smem + off));
}
__device__ __forceinline__ b16x8 mkfrag(s16x4 a, s16x4 b) {
  typedef short s16x8 __attribute__((ext_vector_type(8)));
  s16x8 v = __builtin_shufflevector(a, b, 0, 1, 2, 3, 4, 5, 6, 7);
  return __builtin_bit_cast(b16x8, v);
}
template <bool COH>
__device__ __forceinline__ void glds16(const void* sbase, u32 voff, u32 lds_dst) {
  u32 keep;
  if constexpr (COH)
    asm volatile("s_mov_b32 %0, m0\n\ts_mov_b32 m0, %3\n\ts_nop 0\n\tglobal_load_lds_dwordx4 %1, %2 sc1\n\ts_mov_b32 m0, %0"
                 : "=&s"(keep) : "v"(voff), "s"(sbase), "s"(lds_dst) : "memory");
  else
    asm volatile("s_mov_b32 %0, m0\n\ts_mov_b32 m0, %3\n\ts_nop 0\n\tglobal_load_lds_dwordx4 %1, %2\n\ts_mov_b32 m0, %0"
                 : "=&s"(keep) : "v"(voff), "s"(sbase), "s"(lds_dst) : "memory");
}
__device__ __forceinline__ void glds_touch(const void* g, u32 lds_dst) {
  asm volatile("s_mov_b32 m0, %1\n\ts_nop 0\n\tglobal_load_lds_dword %0, off" :: "v"(g), "s"(lds_dst) : "memory");
}
__device__ __forceinline__ void glds_k(bool same, const void* sbase, u32 voff, u32 m0a, u32 m0b) {
  if (same)
    asm volatile("s_mov_b32 m0, %2\n\ts_nop 0\n\tglobal_load_lds_dwordx4 %0, %1 sc1\n\tglobal_load_lds_dwordx4 %0, %1 offset:1024 sc1"
                 :: "v"(voff), "s"(sbase), "s"(m0a) : "memory");
  else
    asm volatile("s_mov_b32 m0, %2\n\ts_nop 0\n\tglobal_load_lds_dwordx4 %0, %1 sc1\n\ts_mov_b32 m0, %3\n\ts_nop 0\n\tglobal_load_lds_dwordx4 %0, %1 offset:1024 sc1"
                 :: "v"(voff), "s"(sbase), "s"(m0a), "s"(m0b) : "memory");
}
__device__ __forceinline__ bool check_flags256(__amdgpu_buffer_rsrc_t rs, u32 set, int rep, u32 tag, int lane) {
  asm volatile("" ::: "memory");
  u32x4 v = __builtin_amdgcn_raw_buffer_load_b128(rs, set + rep * 8192 + lane * 128, 0, 16);
  return __all(v.x == tag && v.y == tag && v.z == tag && v.w == tag);
}
__device__ __forceinline__ void wait_flags256(__amdgpu_buffer_rsrc_t rs, u32 set, int rep, u32 tag, int lane) {
  for (u32 spins = 0; spins < SPIN_CAP; ++spins) {
    if (check_flags256(rs, set, rep, tag, lane)) break;
    __builtin_amdgcn_s_sleep(2);
  }
  asm volatile("" ::: "memory");
}
__device__ __forceinline__ u32 lds_inc(char* smem, int off, int lane) {
  asm volatile("s_waitcnt lgkmcnt(0)" ::: "memory");
  u32 old = 0;
  if (lane == 0) old = __hip_atomic_fetch_add((LAS u32*)(smem + off), 1u, __ATOMIC_RELAXED, __HIP_MEMORY_SCOPE_WORKGROUP);
  return (u32)__builtin_amdgcn_readfirstlane((int)old);
}
__device__ __forceinline__ void lds_wait(char* smem, int off, u32 n) {
  for (u32 spins = 0; spins < (SPIN_CAP << 3); ++spins) {
    asm volatile("" ::: "memory");
    u32 v = __hip_atomic_load((LAS u32*)(smem + off), __ATOMIC_RELAXED, __HIP_MEMORY_SCOPE_WORKGROUP);
    if ((u32)__builtin_amdgcn_readfirstlane(v) >= n) break;
    __builtin_amdgcn_s_sleep(1);
  }
  asm volatile("" ::: "memory");
}
__device__ __forceinline__ void publish8(char* ws, u32 set, int b, u32 tag, int lane) {
  if (lane < 8) __hip_atomic_store((GAS u32*)(ws + set + lane * 8192 + (b >> 2) * 128 + (b & 3) * 4), tag, __ATOMIC_RELAXED, __HIP_MEMORY_SCOPE_AGENT);
}


template <int C0, int NF, int NBUF>
__device__ __forceinline__ void conv_part(char* smem, int fbase, int gbase, int rh, f32x4 (&acc)[4]) {
#pragma unroll
  for (int c = 0; c < 4; ++c) acc[c] = f32x4{0.f, 0.f, 0.f, 0.f};
  h16x8 Ff[NBUF][NF], Gf[NBUF][NF];
  auto load = [&](int rr, int buf) {
    const int rho = 4 * rh + rr;
    const int fa = fbase - 64 * rho, ga = gbase - 64 * rho;
#pragma unroll
    for (int e = 0; e < NF; ++e) {
      u32x2 lo, hi;
      asm volatile("ds_read_b64 %0, %1 offset:%2" : "=v"(lo) : "v"(fa), "i"(512 * (7 - e)));
      asm volatile("ds_read_b64 %0, %1 offset:%2" : "=v"(hi) : "v"(fa), "i"(512 * (7 - e) + 8));
      Ff[buf][e] = __builtin_bit_cast(h16x8, __builtin_shufflevector(lo, hi, 0, 1, 2, 3));
      Gf[buf][e] = *(const h16x8*)(smem + ga + 512 * e);
    }
  };
  auto mm = [&](int buf) {
#pragma unroll
    for (int c = 0; c < 4; ++c)
#pragma unroll
      for (int qq = 0; qq <= C0 + c; ++qq)
        acc[c] = __builtin_amdgcn_mfma_f32_16x16x32_f16(Ff[buf][qq], Gf[buf][C0 + c - qq], acc[c], 0, 0, 0);
  };
  if constexpr (NBUF == 4) {
    load(0, 0); load(1, 1);
    SCHED();
    WAIT_LGKM0();
    SCHED();
    load(2, 2); load(3, 3);
    SCHED();
    mm(0); mm(1);
    SCHED();
    WAIT_LGKM0();
    SCHED();
    mm(2); mm(3);
  } else {
    load(0, 0);
#pragma unroll
    for (int rr = 0; rr < 4; ++rr) {
      WAIT_LGKM0();
      SCHED();
      if (rr < 3) load(rr + 1, (rr + 1) & 1);
      SCHED();
      mm(rr & 1);
      SCHED();
    }
  }
}

__global__ __launch_bounds__(NT, 2) void mega(Params p) {
  extern __shared__ __attribute__((aligned(16))) char smem[];
  const int tid = threadIdx.x, lane = tid & 63;
  const int w = __builtin_amdgcn_readfirstlane(tid >> 6);
  const int b = blockIdx.x, xcd = b & 7, slot = b >> 3;
  const int lr = lane & 15, lg = lane >> 4;
  const int tq = (lane & 15) >> 2, tp = lane & 3;
  const u32 lbase = (u32)(size_t)(LAS char*)smem;
  const __amdgpu_buffer_rsrc_t rs = mk_rsrc(p.ws, WS_END);
  const int ch0 = 64 * xcd + 2 * slot;
  const int pg = xcd >> 1, fh = xcd & 1, pi = slot & 1, sl = slot >> 1;
  const bool c_active = sl < 15 - fh;
  const int slice = 15 * fh + sl;
  const bool isQ = w >= 4;
  if (tid < 20) *(u32*)(smem + LDS_CNT + tid * 4) = 0u;

  if (w == 2) {
    u64 pc; u32 len;
    asm volatile("s_getpc_b64 %0\n\tmega_code_here:\n\ts_mov_b32 %1, mega_code_end-mega_code_here" : "=s"(pc), "=s"(len));
    const u32 off = (u32)(slot * 16 + (lane & 15) + (lane >> 4) * 512) * 128u;
    const char* a = (const char*)(pc & ~127ull) + (off < len ? off : 0u);
    glds_touch(a, lbase + LDS_JUNK);
  }
  const u32 gen = __hip_atomic_load((GAS u32*)(p.ws + WS_GEN), __ATOMIC_RELAXED, __HIP_MEMORY_SCOPE_AGENT);
  const u32x4 xrow = *(const u32x4*)(p.x + (8 * b + w) * D + 8 * lane);
  u32 xt[4];
  {
    const u16* xs = p.x + (8 * b + 2 * (tid & 3)) * D + 2 * (tid >> 2);
    xt[0] = *(const u32*)(xs); xt[1] = *(const u32*)(xs + D); xt[2] = *(const u32*)(xs + 256); xt[3] = *(const u32*)(xs + 256 + D);
  }
  const int cc = w >> 2, part = ((w >> 1) ^ (w >> 2)) & 1, n = ((w & 1) << 6) | lane;
  const int d = ch0 + cc;
  const u32 v_lar = p.lar[d * NM + n], v_aim = p.aim[d * NM + n], v_cr = p.cr[d * NM + n], v_ci = p.ci[d * NM + n];
  const u32 cr2 = ((const u32*)(p.cr + d * NM))[lane], ci2 = ((const u32*)(p.ci + d * NM))[lane];
  const u32 v_ldt = p.ldt[d];
  const u32 dsk2 = *(const u32*)(p.dsk + ch0);
  SCHED();
  const int kc_ = w >= 3, mt_ = w - 3 * kc_;
  int kcode[4];
#pragma unroll
  for (int r = 0; r < 4; ++r) {
    const int row = 16 * mt_ + 4 * lg + r, j = lr;
    int pos;
    if (row < 16) pos = (row & 7) + 128 * (row >> 3) + 8 * j;
    else if (row < 24) pos = 256 + 2 * (row & 3) + 128 * ((row - 16) >> 2) + 8 * j;
    else if (row < 32) pos = 512 + 4 * (row & 1) + 128 * ((row - 24) >> 1) + 8 * j;
    else pos = 1024 + 128 * (row - 32) + 8 * j;
    const int code = pos == 0 ? 1440 : (int)(__float_as_uint((float)pos) >> 16) - 0x3F80;
    const bool ok = row < 40 || (row == 40 && j == 0);
    kcode[r] = LDS_KC + kc_ * KCCH + (ok ? code : 1442 + lane % 24) * 2;
  }
  u32 ad[7];
#pragma unroll
  for (int e = 0; e < 7; ++e) {
    const u32 fb = __float_as_uint((float)max(2048 - 4 * tid - e, 1));
    ad[e] = (((fb + 0x7FFFu + ((fb >> 16) & 1u)) >> 16) << 1) + (u32)(LDS_KC - 0x3F80 * 2);
  }
  if (tid == 511) { ad[4] = LDS_KC + 1440 * 2; ad[5] = LDS_KC + 1441 * 2; ad[6] = ad[5]; }
  asm volatile("" :: "v"(kcode[0]), "v"(kcode[1]), "v"(kcode[2]), "v"(kcode[3]), "v"(ad[0]), "v"(ad[1]), "v"(ad[2]), "v"(ad[3]), "v"(ad[4]), "v"(ad[5]), "v"(ad[6]));
  SCHED();
  const u32 tag = gen + 1u;
  {
    f32x2 v[4];
#pragma unroll
    for (int i = 0; i < 4; ++i) { const u32 t = xrow[i]; v[i].x = bf2f(t & 0xffffu); v[i].y = bfhi(t); }
    asm volatile("" :: "v"(v_lar), "v"(v_aim), "v"(v_cr), "v"(v_ci), "v"(cr2), "v"(ci2), "v"(v_ldt), "v"(dsk2), "v"(tag), "v"(xt[0]), "v"(xt[1]), "v"(xt[2]), "v"(xt[3]));
#pragma unroll
    for (int ps = 0; ps < 2; ++ps) {
      u32x4 g; g.x = xt[2 * ps]; g.y = tag; g.z = xt[2 * ps + 1]; g.w = tag;
      __builtin_amdgcn_raw_buffer_store_b128(g, rs, WS_XT + ((tid >> 2) + 128 * ps) * XTS + (8 * b + 2 * (tid & 3)) * 8, 0, 16);
    }
    f32x2 s = (v[0] + v[1]) + (v[2] + v[3]);
    const float mean = wave_sum(s.x + s.y) * (1.f / D);
    f32x2 mm; mm.x = mean; mm.y = mean;
    f32x2 s2 = {0.f, 0.f};
#pragma unroll
    for (int i = 0; i < 4; ++i) { f32x2 t = v[i] - mm; s2 = __builtin_elementwise_fma(t, t, s2); }
    const float var = wave_sum(s2.x + s2.y) * (1.f / D);
    const u32 mv = cvt2(var, mean);
    const float den = sqrtf(rbf(bf2f(mv & 0xffffu) + 1e-5f));
    const u32 pay = (mv & 0xffff0000u) | (cvt2(den, den) & 0xffffu);
    if (lane == 0)
      __hip_atomic_store((GAS u64*)(p.ws + WS_STATS) + (8 * b + w), ((u64)tag << 32) | (u64)pay, __ATOMIC_RELAXED, __HIP_MEMORY_SCOPE_AGENT);
  }
  const int gmat = b >> 7, gks = (b >> 3) & 15, gpart = b & 7;
  const int gft0 = gpart < 6 ? 11 * gpart : 10 * gpart + 6, gnft = gpart < 6 ? 11 : 10;
  auto wd_rows = [&](int kap, int& fa, int& fb) {
    const int u = kap - 28;
    fa = kap < 28 ? 48 * kap : (kap < 42 ? 96 * u + 32 : 1344);
    fb = kap < 28 ? 48 * kap + 16 : (kap < 42 ? 96 * u + 80 : 1360);
  };
  if (w == 0) {
    const u16* base = (gmat ? p.wu : p.wg) + (32 * gks) * F;
#pragma unroll
    for (int j = 0; j < 11; ++j) {
      const int nn = 64 * j + lane;
      const int row = nn / 22, c = nn - row * 22;
      glds16<false>(base, (u32)(row * F + min(16 * gft0 + 8 * c, F - 8)) * 2u, lbase + LDS_STG + 8192 + j * 1024);
    }
  } else if (w == 1) {
    const int row = lane >> 3, c16 = lane & 7;
    const u32 vo0 = (u32)(row * D + 16 * ((c16 >> 1) ^ ((row >> 1) & 3)) + 8 * (c16 & 1)) * 2u;
#pragma unroll
    for (int un = 0; un < 2; ++un) {
      const int wunit = b + 256 * un;
      if (wunit < 344) {
        int fa, fb; wd_rows(wunit >> 3, fa, fb);
        const u16* ba = p.wd + fa * D + 64 * (wunit & 7);
        const u16* bb = p.wd + fb * D + 64 * (wunit & 7);
        glds16<false>(ba, vo0, lbase + LDS_STG + un * 4096);
        glds16<false>(ba + 8 * D, vo0, lbase + LDS_STG + un * 4096 + 1024);
        glds16<false>(bb, vo0, lbase + LDS_STG + un * 4096 + 2048);
        glds16<false>(bb + 8 * D, vo0, lbase + LDS_STG + un * 4096 + 3072);
      }
    }
  }
  {
    const float dt = rbf(fast_expf(bf2f(v_ldt)));
    u32 m = max(max(cr2 & 0x7fffu, (cr2 >> 16) & 0x7fffu), max(ci2 & 0x7fffu, (ci2 >> 16) & 0x7fffu));
    m = wave_maxu(m);
    int ebound = ((int)(m >> 7) - 126) + ((int)((__float_as_uint(dt) >> 23) & 0xffu) - 126);
    ebound = max(-120, min(120, ebound));
    if ((w & 3) == 0 && lane == 0) *(float*)(smem + LDS_SC + cc * 4) = ldexpf(1.f, ebound - 6);
    const float a_r = -rbf(fast_expf(bf2f(v_lar))) * dt, a_i = bf2f(v_aim) * dt;
    const float ar2 = a_r * 1.44269504088896341f, airc = -a_i * 0.15915494309189535f;
    const int tb = LDS_TAB + cc * TABCH + n * 8;
    f32x2 Cc; Cc.x = ldexpf(bf2f(v_cr) * dt, 10 - ebound); Cc.y = -ldexpf(bf2f(v_ci) * dt, 10 - ebound);
#define PUTX(row, v) put_tab<false>(smem, tb + (row) * XROW, v)
#define PUTY(col, v) put_tab<true>(smem, tb + (41 + (col)) * XROW, v)
    if (part == 0) {
      f32x2 E0 = cexp_k(ar2, airc, 1.f), E1 = cmul(E0, E0), E2 = cmul(E1, E1), E7 = cexp_k(ar2, airc, 128.f), E8 = cmul(E7, E7);
      f32x2 T[8];
      T[0] = Cc; T[1] = cmul(E0, Cc); T[2] = cmul(E1, Cc); T[3] = cmul(E1, T[1]);
      T[4] = cmul(E2, Cc); T[5] = cmul(E2, T[1]); T[6] = cmul(E2, T[2]); T[7] = cmul(E2, T[3]);
#pragma unroll
      for (int e = 0; e < 8; ++e) { PUTX(e, T[e]); PUTX(8 + e, cmul(E7, T[e])); }
      f32x2 S[4];
      S[0] = cmul(E8, Cc); S[1] = cmul(E1, S[0]); S[2] = cmul(E2, S[0]); S[3] = cmul(E2, S[1]);
#pragma unroll
      for (int e = 0; e < 4; ++e) { PUTX(16 + e, S[e]); PUTX(20 + e, cmul(E7, S[e])); }
    } else {
      f32x2 E2 = cexp_k(ar2, airc, 4.f), E3 = cmul(E2, E2), E4 = cmul(E3, E3), E5 = cmul(E4, E4);
      f32x2 E6 = cexp_k(ar2, airc, 64.f), E7 = cmul(E6, E6), E8 = cmul(E7, E7), E9 = cmul(E8, E8);
      f32x2 E10 = cexp_k(ar2, airc, 1024.f), E11 = cmul(E10, E10);
      f32x2 Y[8];
      Y[0] = f32x2{1.f, 0.f}; Y[1] = E3; Y[2] = E4; Y[3] = cmul(E3, E4);
      Y[4] = E5; Y[5] = cmul(E5, E3); Y[6] = cmul(E5, E4); Y[7] = cmul(E5, Y[3]);
#pragma unroll
      for (int j = 0; j < 8; ++j) { PUTY(j, Y[j]); f32x2 t = (j == 0) ? E6 : cmul(E6, Y[j]); PUTY(8 + j, t); }
      f32x2 G[8];
      G[1] = E7; G[2] = E8; G[3] = cmul(E7, E8); G[4] = E9; G[5] = cmul(E9, E7); G[6] = cmul(E9, E8); G[7] = cmul(E9, G[3]);
      f32x2 V0 = cmul(E9, Cc), V1 = cmul(E2, V0);
      PUTX(24, V0); PUTX(25, V1);
#pragma unroll
      for (int i = 1; i < 4; ++i) { PUTX(24 + 2 * i, cmul(V0, G[i])); PUTX(25 + 2 * i, cmul(V1, G[i])); }
      f32x2 W0 = cmul(E10, Cc);
      PUTX(32, W0);
#pragma unroll
      for (int i = 1; i < 8; ++i) PUTX(32 + i, cmul(W0, G[i]));
      PUTX(40, cmul(E11, Cc));
    }
  }
  if (w == 0) {
    WAIT_VM(0);
    const int ro = LDS_STG + 8192 + (4 * lg + tq) * 352 + tp * 8;
#pragma unroll
    for (int t = 0; t < 11; ++t) {
      if (t < gnft) {
        s16x4 a = tr_read(smem, ro + t * 32), c = tr_read(smem, ro + t * 32 + 16 * 352);
        const int ftile = gft0 + t, sl3 = ftile / 3, t6 = gmat * 3 + (ftile - 3 * sl3);
        __builtin_amdgcn_raw_buffer_store_b128(__builtin_bit_cast(u32x4, mkfrag(a, c)), rs, WS_WT + ((sl3 * 16 + gks) * 6 + t6) * 1024 + lane * 16, 0, 16);
      }
    }
  } else if (w == 1) {
    WAIT_VM(0);
    const int row = 4 * lg + tq;
#pragma unroll
    for (int un = 0; un < 2; ++un) {
      const int wunit = b + 256 * un;
      if (wunit < 344) {
#pragma unroll
        for (int dtl = 0; dtl < 4; ++dtl) {
          const int o = LDS_STG + un * 4096 + row * 128 + ((dtl ^ ((row >> 1) & 3)) * 32) + tp * 8;
          s16x4 a = tr_read(smem, o), c = tr_read(smem, o + 2048);
          __builtin_amdgcn_raw_buffer_store_b128(__builtin_bit_cast(u32x4, mkfrag(a, c)), rs, WS_WD + (((wunit >> 3) * 32 + (wunit & 7) * 4 + dtl) * 64 + lane) * 16, 0, 16);
        }
      }
    }
  }
  const u32 xto = WS_XT + (u32)(ch0 >> 1) * XTS + tid * 32, sto = WS_STATS + tid * 32;
  u32x4 gx0 = __builtin_amdgcn_raw_buffer_load_b128(rs, xto, 0, 16), gx1 = __builtin_amdgcn_raw_buffer_load_b128(rs, xto + 16, 0, 16);
  u32x4 gs0 = __builtin_amdgcn_raw_buffer_load_b128(rs, sto, 0, 16), gs1 = __builtin_amdgcn_raw_buffer_load_b128(rs, sto + 16, 0, 16);
  u32 unb[4], xcol[4];
  auto gathered = [&]() { return __all(gx0.y == tag && gx0.w == tag && gx1.y == tag && gx1.w == tag && gs0.y == tag && gs0.w == tag && gs1.y == tag && gs1.w == tag); };
  auto regather = [&]() {
    const bool ok = gx0.y == tag && gx0.w == tag && gx1.y == tag && gx1.w == tag && gs0.y == tag && gs0.w == tag && gs1.y == tag && gs1.w == tag;
    asm volatile("" ::: "memory");
    if (!ok) {
      gx0 = __builtin_amdgcn_raw_buffer_load_b128(rs, xto, 0, 16); gx1 = __builtin_amdgcn_raw_buffer_load_b128(rs, xto + 16, 0, 16);
      gs0 = __builtin_amdgcn_raw_buffer_load_b128(rs, sto, 0, 16); gs1 = __builtin_amdgcn_raw_buffer_load_b128(rs, sto + 16, 0, 16);
    }
  };
  auto normalise = [&]() {
    xcol[0] = gx0.x; xcol[1] = gx0.z; xcol[2] = gx1.x; xcol[3] = gx1.z;
    const u32 st[4] = {gs0.x, gs0.z, gs1.x, gs1.z};
    u32 uh0[2], uh1[2];
#pragma unroll
    for (int i = 0; i < 4; ++i) {
      const float m = bfhi(st[i]), den = bf2f(st[i] & 0xffffu);
      const float rc = __builtin_amdgcn_rcpf(den);
      const u32 ab = cvt2(bf2f(xcol[i] & 0xffffu) - m, bfhi(xcol[i]) - m);
      f32x2 a; a.x = bf2f(ab & 0xffffu); a.y = bfhi(ab);
      f32x2 rc2 = {rc, rc}, nden = {-den, -den};
      f32x2 qv = a * rc2;
      qv = __builtin_elementwise_fma(__builtin_elementwise_fma(qv, nden, a), rc2, qv);
      unb[i] = cvt2(qv.x, qv.y);
      const h16x2 hp = __builtin_bit_cast(h16x2, __builtin_amdgcn_cvt_pkrtz(bf2f(unb[i] & 0xffffu), bfhi(unb[i])));
      const u32 hb2 = __builtin_bit_cast(u32, hp);
      if (i & 1) { uh0[i >> 1] = (uh0[i >> 1] & 0xffffu) | (hb2 << 16); uh1[i >> 1] = (uh1[i >> 1] >> 16) | (hb2 & 0xffff0000u); }
      else { uh0[i >> 1] = hb2; uh1[i >> 1] = hb2; }
    }
    const int uo = LDS_U + (240 + 4 * tid) * 2;
    u32x2 v0; v0.x = uh0[0]; v0.y = uh0[1];
    u32x2 v1; v1.x = uh1[0]; v1.y = uh1[1];
    *(u32x2*)(smem + uo) = v0;
    *(u32x2*)(smem + uo + UCH) = v1;
  };
  if (tid < 120) { *(u32*)(smem + LDS_U + tid * 4) = 0u; *(u32*)(smem + LDS_U + UCH + tid * 4) = 0u; }
  BARRIER();
  if (w < 6) {
    const int xa = LDS_TAB + kc_ * TABCH + (16 * mt_ + lr) * XROW + lg * 16;
    const int ya = LDS_TAB + kc_ * TABCH + (41 + lr) * XROW + lg * 16;
    f32x4 a0 = {0.f, 0.f, 0.f, 0.f}, a1 = {0.f, 0.f, 0.f, 0.f};
    u32x4 xa4[16], yb4[16];
#pragma unroll
    for (int ks = 0; ks < 16; ++ks) { xa4[ks] = *(const u32x4*)(smem + xa + ks * 64); yb4[ks] = *(const u32x4*)(smem + ya + ks * 64); }
    SCHED();
#pragma unroll
    for (int ks = 0; ks < 16; ++ks) {
      u32x4 ys4; ys4.x = yb4[ks].y; ys4.y = yb4[ks].x; ys4.z = yb4[ks].w; ys4.w = yb4[ks].z;
      a0 = __builtin_amdgcn_mfma_f32_16x16x32_f16(__builtin_bit_cast(h16x8, xa4[ks]), __builtin_bit_cast(h16x8, yb4[ks]), a0, 0, 0, 0);
      a1 = __builtin_amdgcn_mfma_f32_16x16x32_f16(__builtin_bit_cast(h16x8, xa4[ks]), __builtin_bit_cast(h16x8, ys4), a1, 0, 0, 0);
    }
#pragma unroll
    for (int r = 0; r < 4; ++r) *(_Float16*)(smem + kcode[r]) = (_Float16)((a0[r] + a1[r]) * 0.125f);
    if (mt_ == 0 && lane == 0) *(_Float16*)(smem + LDS_KC + kc_ * KCCH + 1441 * 2) = (_Float16)0.f;
  }
  if (w < 2) {
    WAIT_VM(4);
    publish8(p.ws, w == 0 ? WS_FLAGW : WS_FLAGW2, b, tag, lane);
  }
  BARRIER();
  {
    u32 r[2][7];
#pragma unroll
    for (int kc = 0; kc < 2; ++kc)
#pragma unroll
      for (int e = 0; e < 7; ++e) r[kc][e] = *(const u16*)(smem + kc * KCCH + ad[e]);
    u32 k0 = 0;
    if (tid < 32) k0 = *(const u16*)(smem + LDS_KC + (tid >> 4) * KCCH + 1440 * 2);
    SCHED();
#pragma unroll
    for (int kc = 0; kc < 2; ++kc) {
      const int fo = LDS_F + kc * 4 * FCOPY + tid * 8;
      const u32 p01 = r[kc][0] | (r[kc][1] << 16), p23 = r[kc][2] | (r[kc][3] << 16), p45 = r[kc][4] | (r[kc][5] << 16);
      const u32 p12 = r[kc][1] | (r[kc][2] << 16), p34 = r[kc][3] | (r[kc][4] << 16), p56 = r[kc][5] | (r[kc][6] << 16);
      *(u32x2*)(smem + fo) = u32x2{p01, p23};
      *(u32x2*)(smem + fo + FCOPY) = u32x2{p12, p34};
      *(u32x2*)(smem + fo + 2 * FCOPY) = u32x2{p23, p45};
      *(u32x2*)(smem + fo + 3 * FCOPY) = u32x2{p34, p56};
    }
    if (tid < 32) {
      const int kc = tid >> 4, s = (tid >> 2) & 3, mm = 512 + (tid & 3);
      *(u32x2*)(smem + LDS_F + kc * 4 * FCOPY + s * FCOPY + mm * 8) = u32x2{(s == 0 && mm == 512) ? k0 : 0u, 0u};
    }
  }
  {
    for (u32 spins = 0; spins < SPIN_CAP; ++spins) {
      if (gathered()) break;
      __builtin_amdgcn_s_sleep(1);
      regather();
    }
    asm volatile("" ::: "memory");
    normalise();
  }
  BARRIER();
  if (b == 0 && tid == 0)
    __hip_atomic_store((GAS u32*)(p.ws + WS_GEN), tag, __ATOMIC_RELAXED, __HIP_MEMORY_SCOPE_AGENT);

  const int rot = (sl * 5 + pi * 8) & 15;
  const float sc0 = *(const float*)(smem + LDS_SC), sc1 = *(const float*)(smem + LDS_SC + 4);
  auto epilogue = [&]() {
    const int l0 = 4 * (tid & 255);
    const int c = l0 >> 8, i = (l0 >> 4) & 15, g = tid & 3;
    const int ro = LDS_PART + c * 1024 + (16 * g + ((i + 4 * g) & 15)) * 16;
    const float dk0 = bf2f(dsk2 & 0xffffu), dk1 = bfhi(dsk2);
    u32 hw[2][2];
    const f32x4 y00 = *(const f32x4*)(smem + ro), y01 = *(const f32x4*)(smem + ro + 4096), y10 = *(const f32x4*)(smem + ro + 8192), y11 = *(const f32x4*)(smem + ro + 12288);
    SCHED();
#pragma unroll
    for (int k2 = 0; k2 < 2; ++k2) {
      const f32x4 y = k2 ? (y10 + y11) * sc1 : (y00 + y01) * sc0;
      const float dk = k2 ? dk1 : dk0;
#pragma unroll
      for (int pr = 0; pr < 2; ++pr) {
        const u32 u0 = unb[2 * pr], u1 = unb[2 * pr + 1], x0 = xcol[2 * pr], x1 = xcol[2 * pr + 1];
        const u32 yb = cvt2(y[2 * pr], y[2 * pr + 1]);
        const float un0 = k2 ? bfhi(u0) : bf2f(u0 & 0xffffu), un1 = k2 ? bfhi(u1) : bf2f(u1 & 0xffffu);
        const u32 sk = cvt2(un0 * dk, un1 * dk);
        const u32 ab = cvt2(bf2f(yb & 0xffffu) + bf2f(sk & 0xffffu), bfhi(yb) + bfhi(sk));
        const float xv0 = k2 ? bfhi(x0) : bf2f(x0 & 0xffffu), xv1 = k2 ? bfhi(x1) : bf2f(x1 & 0xffffu);
        hw[k2][pr] = cvt2(xv0 + bf2f(ab & 0xffffu), xv1 + bfhi(ab));
      }
    }
    const bool odd = tid & 1;
    const u32 s0 = odd ? hw[0][0] : hw[1][0], s1 = odd ? hw[0][1] : hw[1][1];
    const u32 r0 = (u32)__builtin_amdgcn_update_dpp(0, (int)s0, 0xB1, 0xf, 0xf, true), r1 = (u32)__builtin_amdgcn_update_dpp(0, (int)s1, 0xB1, 0xf, 0xf, true);
    u32x4 o;
    if (odd) { o.x = r0; o.y = r1; o.z = hw[1][0]; o.w = hw[1][1]; }
    else { o.x = hw[0][0]; o.y = hw[0][1]; o.z = r0; o.w = r1; }
    const int dd = ch0 + (odd ? 1 : 0), ll = 4 * (tid & ~1);
    __builtin_amdgcn_raw_buffer_store_b128(o, rs, WS_HT + ((ll >> 5) * D + dd) * 64 + (ll & 31) * 2, 0, 16);
  };

  if (isQ) {
    const int qw = w - 4, kc = qw >> 1, rh = qw & 1;
    const int sft = (4 - (lr & 3)) & 3;
    const int fbase = (int)lbase + LDS_F + kc * 4 * FCOPY + sft * FCOPY + 2 * (2032 + 8 * lg - (lr + sft)) - 512 * 7;
    const int gbase = LDS_U + kc * UCH + 2 * (224 + 16 * lr + 8 * lg);
    const int po = LDS_PART + qw * 4096 + (16 * lg + ((lr + 4 * lg) & 15)) * 16;
    f32x4 acc[4];
    conv_part<0, 4, 4>(smem, fbase, gbase, rh, acc);
#pragma unroll
    for (int c = 0; c < 4; ++c) *(f32x4*)(smem + po + c * 1024) = acc[c];
    lds_inc(smem, LDS_CNT + 32, lane);
    conv_part<4, 8, 2>(smem, fbase, gbase, rh, acc);
    lds_inc(smem, LDS_CNT + 36, lane);
    lds_wait(smem, LDS_CNT + 8, 4);
#pragma unroll
    for (int c = 0; c < 4; ++c) *(f32x4*)(smem + po + c * 1024) = acc[c];
    lds_inc(smem, LDS_CNT + 40, lane);
    lds_wait(smem, LDS_CNT + 40, 4);
    epilogue();
    WAIT_VM(0);
    if (lds_inc(smem, LDS_CNT + 0, lane) == 3) publish8(p.ws, WS_FLAGB + FSET, b, tag, lane);
    lds_wait(smem, LDS_CNT + 0, 4);
    if (c_active) lds_wait(smem, LDS_CNT + 4, 4);
  } else {
    if (c_active) {
      wait_flags256(rs, WS_FLAGW, xcd, tag, lane);
      const char* wsrc = p.ws + WS_WT + slice * 98304 + w * 24576;
#pragma unroll
      for (int jj = 0; jj < 24; ++jj) glds16<true>(wsrc + jj * 1024, (u32)lane * 16u, lbase + LDS_W + w * 24576 + jj * 1024);
    }
    lds_wait(smem, LDS_CNT + 32, 4);
    epilogue();
    lds_inc(smem, LDS_CNT + 8, lane);
    WAIT_VM(0);
    if (lds_inc(smem, LDS_CNT + 52, lane) == 3) publish8(p.ws, WS_FLAGB, b, tag, lane);
    WAIT_VM(0);
    lds_inc(smem, LDS_CNT + 48, lane);
    if (c_active) {
      lds_wait(smem, LDS_CNT + 48, 4);
      for (int tl = w; tl < 6; tl += 4) {
        u32x4 one4 = {0x3F803F80u, 0x3F803F80u, 0x3F803F80u, 0x3F803F80u};
        f32x4 a = {0.f, 0.f, 0.f, 0.f};
        b16x8 wf[16];
#pragma unroll
        for (int ks = 0; ks < 16; ++ks) wf[ks] = *(const b16x8*)(smem + LDS_W + (ks * 6 + tl) * 1024 + lane * 16);
        SCHED();
#pragma unroll
        for (int ks = 0; ks < 16; ++ks) a = __builtin_amdgcn_mfma_f32_16x16x32_bf16(wf[ks], __builtin_bit_cast(b16x8, one4), a, 0, 0, 0);
        if (lr == 0) *(f32x4*)(smem + LDS_CS + (tl * 16 + 4 * lg) * 4) = a;
      }
      lds_inc(smem, LDS_CNT + 4, lane);
      lds_wait(smem, LDS_CNT + 36, 4);
    }
  }

  if (c_active) {
    const int grp = w >> 2, gw = w & 3;
    const int l0 = 1024 * grp + 256 * pg + 128 * pi + 32 * gw;
    const int ring = grp ? (LDS_RINGQ + gw * 7168) : (LDS_RINGP + gw * 8192);
    if (gw == 0) { wait_flags256(rs, WS_FLAGB + grp * FSET, xcd, tag, lane); lds_inc(smem, LDS_CNT + 12 + grp * 4, lane); }
    else lds_wait(smem, LDS_CNT + 12 + grp * 4, 1);
    const int drow = lane >> 2, dc16 = lane & 3;
    const u32 hvoff = (u32)(drow * 64 + ((dc16 >> 1) ^ ((drow >> 2) & 1)) * 32 + (dc16 & 1) * 16);
    const char* hbase = p.ws + WS_HT + (l0 >> 5) * 32768;
#define HDMA(j) glds_k(((2 * (j)) % 7) != 6, hbase + (((j) + rot) & 15) * 2048, hvoff, lbase + ring + ((2 * (j)) % 7) * 1024, lbase + ring - 1024)
    HDMA(0); HDMA(1); HDMA(2);
    const int hlane = ring + (4 * lg + tq) * 64 + tp * 8;
    const int hx0 = ((0 ^ (lg & 1)) * 32), hx1 = ((1 ^ (lg & 1)) * 32);
    f32x4 ag[3][2], au[3][2];
#pragma unroll
    for (int ft = 0; ft < 3; ++ft)
#pragma unroll
      for (int lt = 0; lt < 2; ++lt) { ag[ft][lt] = f32x4{0.f, 0.f, 0.f, 0.f}; au[ft][lt] = f32x4{0.f, 0.f, 0.f, 0.f}; }
    u32x2 st2[2];
    const u32 st2o = WS_STAT2 + (l0 + lr) * 8;
    b16x8 wgf[2][3], wuf[2][3], hf[2][2];
    auto rdw = [&](int i, int buf) {
      const int wa = LDS_W + lane * 16 + ((i + rot) & 15) * 6144;
#pragma unroll
      for (int ft = 0; ft < 3; ++ft) {
        wgf[buf][ft] = *(const b16x8*)(smem + wa + ft * 1024);
        wuf[buf][ft] = *(const b16x8*)(smem + wa + (3 + ft) * 1024);
      }
    };
    auto rdh = [&](int i, int buf) {
      const int sa = hlane + ((2 * i) % 7) * 1024, sb = hlane + ((2 * i + 1) % 7) * 1024;
      hf[buf][0] = mkfrag(tr_read(smem, sa + hx0), tr_read(smem, sb + hx0));
      hf[buf][1] = mkfrag(tr_read(smem, sa + hx1), tr_read(smem, sb + hx1));
    };
    rdw(0, 0);
    WAIT_VM(4);
    rdh(0, 0);
#pragma unroll
    for (int i = 0; i < 16; ++i) {
      const int cur = i & 1;
      WAIT_LGKM0();
      SCHED();
      if (i + 3 < 16) HDMA(i + 3);
      if (i < 15) rdw(i + 1, cur ^ 1);
      SCHED();
#pragma unroll
      for (int ft = 0; ft < 3; ++ft) {
        ag[ft][0] = __builtin_amdgcn_mfma_f32_16x16x32_bf16(wgf[cur][ft], hf[cur][0], ag[ft][0], 0, 0, 0);
        au[ft][0] = __builtin_amdgcn_mfma_f32_16x16x32_bf16(wuf[cur][ft], hf[cur][0], au[ft][0], 0, 0, 0);
      }
      SCHED();
      if (i < 15) {
        if (i <= 12) WAIT_VM(4); else if (i == 13) WAIT_VM(2); else WAIT_VM(0);
        rdh(i + 1, cur ^ 1);
      } else {
        st2[0] = __builtin_amdgcn_raw_buffer_load_b64(rs, st2o, 0, 16); st2[1] = __builtin_amdgcn_raw_buffer_load_b64(rs, st2o + 128, 0, 16);
      }
      SCHED();
#pragma unroll
      for (int ft = 0; ft < 3; ++ft) {
        ag[ft][1] = __builtin_amdgcn_mfma_f32_16x16x32_bf16(wgf[cur][ft], hf[cur][1], ag[ft][1], 0, 0, 0);
        au[ft][1] = __builtin_amdgcn_mfma_f32_16x16x32_bf16(wuf[cur][ft], hf[cur][1], au[ft][1], 0, 0, 0);
      }
      SCHED();
    }
    if (!isQ) lds_wait(smem, LDS_CNT + 4, 4);
    for (u32 spins = 0; spins < SPIN_CAP; ++spins) {
      const bool ok = st2[0].y == tag && st2[1].y == tag;
      if (__all(ok)) break;
      __builtin_amdgcn_s_sleep(2);
      asm volatile("" ::: "memory");
      st2[0] = __builtin_amdgcn_raw_buffer_load_b64(rs, st2o, 0, 16); st2[1] = __builtin_amdgcn_raw_buffer_load_b64(rs, st2o + 128, 0, 16);
    }
    u32 hp[3][2][2];
    f32x4 cgv[3], cuv[3];
#pragma unroll
    for (int ft = 0; ft < 3; ++ft) { cgv[ft] = *(const f32x4*)(smem + LDS_CS + (ft * 16 + 4 * lg) * 4); cuv[ft] = *(const f32x4*)(smem + LDS_CS + (48 + ft * 16 + 4 * lg) * 4); }
    SCHED();
#pragma unroll
    for (int lt = 0; lt < 2; ++lt) {
      const u32 mv = st2[lt].x;
      const float m = bfhi(mv), den = bf2f(mv & 0xffffu);
      const float rinv = __builtin_amdgcn_rcpf(den) ;
      const f32x2 nm2 = {-m, -m}, ri2 = {rinv, rinv};
#pragma unroll
      for (int ft = 0; ft < 3; ++ft) {
        const f32x4 cg = cgv[ft], cu = cuv[ft];
#pragma unroll
        for (int pr = 0; pr < 2; ++pr) {
          f32x2 g2 = {ag[ft][lt][2 * pr], ag[ft][lt][2 * pr + 1]}, u2 = {au[ft][lt][2 * pr], au[ft][lt][2 * pr + 1]};
          f32x2 cg2 = {cg[2 * pr], cg[2 * pr + 1]}, cu2 = {cu[2 * pr], cu[2 * pr + 1]};
          g2 = __builtin_elementwise_fma(nm2, cg2, g2) * ri2;
          u2 = __builtin_elementwise_fma(nm2, cu2, u2) * ri2;
          const u32 gb = cvt2(g2.x, g2.y), ub = cvt2(u2.x, u2.y);
          f32x2 gr = {fmaxf(bf2f(gb & 0xffffu), 0.f), fmaxf(bfhi(gb), 0.f)}, ur = {bf2f(ub & 0xffffu), bfhi(ub)};
          gr = gr * ur;
          hp[ft][lt][pr] = cvt2(gr.x, gr.y);
        }
      }
    }
    const int lt0 = l0 >> 4;
    if (slice < 28) {
#pragma unroll
      for (int lt = 0; lt < 2; ++lt) {
        u32x4 v; v.x = hp[0][lt][0]; v.y = hp[0][lt][1]; v.z = hp[1][lt][0]; v.w = hp[1][lt][1];
        __builtin_amdgcn_raw_buffer_store_b128(v, rs, WS_PB + ((slice * 128 + lt0 + lt) * 64 + lane) * 16, 0, 16);
      }
      u32x4 v; v.x = hp[2][0][0]; v.y = hp[2][0][1]; v.z = hp[2][1][0]; v.w = hp[2][1][1];
      __builtin_amdgcn_raw_buffer_store_b128(v, rs, WS_SB + ((slice * 64 + (lt0 >> 1)) * 64 + lane) * 16, 0, 16);
    } else {
#pragma unroll
      for (int ft = 0; ft < 2; ++ft) {
        u32x4 v; v.x = hp[ft][0][0]; v.y = hp[ft][0][1]; v.z = hp[ft][1][0]; v.w = hp[ft][1][1];
        __builtin_amdgcn_raw_buffer_store_b128(v, rs, WS_SB + (((28 + ft) * 64 + (lt0 >> 1)) * 64 + lane) * 16, 0, 16);
      }
    }
    WAIT_VM(0);
    if (lane == 0)
      __hip_atomic_store((GAS u32*)(p.ws + WS_FLAGH + (grp * 32 + ((l0 >> 5) & 31)) * 1024 + (slice >> 2) * 128 + (slice & 3) * 4), tag, __ATOMIC_RELAXED, __HIP_MEMORY_SCOPE_AGENT);
  } else {
    const int sidx = fh ? 8 + pg * 4 + (slot - 28) : pg * 2 + (slot - 30);
    {
      const int half = w >> 2, hw = w & 3;
      const int g0 = 32 * half + sidx, g1 = 32 * half + 24 + (half ? sidx - 16 : sidx);
      const bool two = half ? sidx >= 16 : sidx < 8;
      if (hw == 0) { wait_flags256(rs, WS_FLAGB + half * FSET, xcd, tag, lane); lds_inc(smem, LDS_CNT + 12 + 4 * half, lane); }
      else lds_wait(smem, LDS_CNT + 12 + 4 * half, 1);
      float* sacc = (float*)(smem + LDS_P2 + half * 8192);
#pragma unroll 1
      for (int gg = 0; gg < (two ? 2 : 1); ++gg) {
        u32x4 v[8];
#pragma unroll
        for (int j = 0; j < 8; ++j)
          v[j] = __builtin_amdgcn_raw_buffer_load_b128(rs, WS_HT + (gg ? g1 : g0) * 32768 + (128 * hw + 16 * j) * 64 + lane * 16, 0, 16);
        SCHED();
        f32x2 a1[4], a2[4];
#pragma unroll
        for (int e = 0; e < 4; ++e) { a1[e] = f32x2{0.f, 0.f}; a2[e] = f32x2{0.f, 0.f}; }
#pragma unroll
        for (int j = 0; j < 8; ++j)
#pragma unroll
          for (int e = 0; e < 4; ++e) {
            const u32 t = v[j][e];
            const f32x2 x2 = {bf2f(t & 0xffffu), bfhi(t)};
            a1[e] += x2; a2[e] = __builtin_elementwise_fma(x2, x2, a2[e]);
          }
#pragma unroll
        for (int e = 0; e < 4; ++e)
#pragma unroll
          for (int hh = 0; hh < 2; ++hh) {
            float s = a1[e][hh], q2 = a2[e][hh];
            s = dpp_addf<0x124, 0xf>(s); s = dpp_addf<0x128, 0xf>(s); s = sum_rows(s);
            q2 = dpp_addf<0x124, 0xf>(q2); q2 = dpp_addf<0x128, 0xf>(q2); q2 = sum_rows(q2);
            if (lane < 4) { f32x2 o = {s, q2}; *(f32x2*)(sacc + ((gg * 4 + hw) * 32 + 8 * lane + 2 * e + hh) * 2) = o; }
          }
        const u32 cnt = LDS_CNT + 56 + 4 * (2 * half + gg) - 0;
        if (lds_inc(smem, cnt, lane) == 3) {
          if (lane < 32) {
            f32x2 t = {0.f, 0.f};
#pragma unroll
            for (int k4 = 0; k4 < 4; ++k4) t += *(const f32x2*)(sacc + ((gg * 4 + k4) * 32 + lane) * 2);
            const float mean = t.x * (1.f / D);
            const float var = fmaxf(t.y * (1.f / D) - mean * mean, 0.f);
            const u32 mv = cvt2(var, mean);
            const float den = sqrtf(rbf(bf2f(mv & 0xffffu) + 1e-5f));
            const u32 pay = (mv & 0xffff0000u) | (cvt2(den, den) & 0xffffu);
            __hip_atomic_store((GAS u64*)(p.ws + WS_STAT2) + (gg ? g1 : g0) * 32 + lane, ((u64)tag << 32) | (u64)pay, __ATOMIC_RELAXED, __HIP_MEMORY_SCOPE_AGENT);
          }
        }
      }
    }
  }

  {
    const int rg = slot >> 3, dg = slot & 7;
    const int pair0 = isQ ? 16 + 3 * (w - 4) : 4 * w;
    const int sing0 = isQ ? 4 + 3 * (w - 4) : w;
    wait_flags256(rs, WS_FLAGW2, xcd, tag, lane);
    u32x4 wd[6][4];
#pragma unroll
    for (int i = 0; i < 6; ++i) {
      int kap;
      if (isQ) kap = i < 3 ? pair0 + i : min(28 + sing0 + (i - 3), 42);
      else kap = i < 4 ? pair0 + i : 28 + sing0;
#pragma unroll
      for (int dtl = 0; dtl < 4; ++dtl)
        if (isQ || i < 5) wd[i][dtl] = __builtin_amdgcn_raw_buffer_load_b128(rs, WS_WD + ((kap * 32 + dg * 4 + dtl) * 64 + lane) * 16, 0, 16);
    }
#pragma unroll
    for (int st = 0; st < 2; ++st) {
      const int l0 = 1024 * st + 128 * xcd + 32 * rg;
      const int lt0 = l0 >> 4;
      if (w == 0) {
        wait_flags256(rs, WS_FLAGB + st * FSET, xcd, tag, lane);
        const u32 fo = WS_FLAGH + (st * 32 + 4 * xcd + rg) * 1024 + (lane & 7) * 128;
        for (u32 spins = 0; spins < SPIN_CAP; ++spins) {
          asm volatile("" ::: "memory");
          u32x4 v = __builtin_amdgcn_raw_buffer_load_b128(rs, fo, 0, 16);
          const bool last = (lane & 7) == 7;
          bool ok = v.x == tag && (last || (v.y == tag && v.z == tag && v.w == tag));
          if (__all(ok)) break;
          __builtin_amdgcn_s_sleep(2);
        }
        lds_inc(smem, LDS_CNT + 20 + st * 4, lane);
      } else lds_wait(smem, LDS_CNT + 20 + st * 4, 1);
      if (st == 0) BARRIER();
      if (w < 4)
        glds16<true>(p.ws + WS_HT + (l0 >> 5) * 32768 + (64 * dg) * 64 + w * 1024, (u32)lane * 16u, lbase + LDS_RES + st * 4096 + w * 1024);
      u32x4 ha[6], hb[6];
      const u32 pbase = WS_PB + ((pair0 * 128 + lt0) * 64 + lane) * 16;
      const u32 sbase = WS_SB + ((2 * sing0 * 64 + (lt0 >> 1)) * 64 + lane) * 16;
#pragma unroll
      for (int i = 0; i < 6; ++i) {
        const bool is_pair = isQ ? i < 3 : i < 4;
        const int si = isQ ? i - 3 : i - 4;
        if (isQ || i < 5) {
          u32 oa, ob;
          if (is_pair) { oa = pbase + i * 131072; ob = oa + 1024; }
          else { oa = sbase + ((w == 7 && si == 2) ? 1 : si) * 131072; ob = oa + 65536; }
          ha[i] = __builtin_amdgcn_raw_buffer_load_b128(rs, oa, 0, 16);
          hb[i] = __builtin_amdgcn_raw_buffer_load_b128(rs, ob, 0, 16);
        }
      }
      SCHED();
      f32x4 acc[4][2];
#pragma unroll
      for (int dtl = 0; dtl < 4; ++dtl) { acc[dtl][0] = f32x4{0.f, 0.f, 0.f, 0.f}; acc[dtl][1] = f32x4{0.f, 0.f, 0.f, 0.f}; }
#pragma unroll
      for (int i = 0; i < 6; ++i) {
        const bool is_pair = isQ ? i < 3 : i < 4;
        if (isQ || i < 5) {
          u32x4 f0, f1;
          if (is_pair) { f0 = ha[i]; f1 = hb[i]; }
          else {
            f0.x = ha[i].x; f0.y = ha[i].y; f0.z = hb[i].x; f0.w = hb[i].y;
            f1.x = ha[i].z; f1.y = ha[i].w; f1.z = hb[i].z; f1.w = hb[i].w;
          }
          if (i == 5 && w == 7) { f0 = u32x4{0u, 0u, 0u, 0u}; f1 = f0; }
#pragma unroll
          for (int dtl = 0; dtl < 4; ++dtl) {
            acc[dtl][0] = __builtin_amdgcn_mfma_f32_16x16x32_bf16(__builtin_bit_cast(b16x8, wd[i][dtl]), __builtin_bit_cast(b16x8, f0), acc[dtl][0], 0, 0, 0);
            acc[dtl][1] = __builtin_amdgcn_mfma_f32_16x16x32_bf16(__builtin_bit_cast(b16x8, wd[i][dtl]), __builtin_bit_cast(b16x8, f1), acc[dtl][1], 0, 0, 0);
          }
        }
      }
#pragma unroll
      for (int dtl = 0; dtl < 4; ++dtl)
#pragma unroll
        for (int lt = 0; lt < 2; ++lt)
          *(f32x4*)(smem + LDS_P2 + st * 65536 + ((w * 4 + dtl) * 2 + lt) * 1024 + lane * 16) = acc[dtl][lt];
      WAIT_VM(0);
      BARRIER();
      {
        const int dtl = w >> 1, lt = w & 1;
        f32x4 pv[8];
#pragma unroll
        for (int k8 = 0; k8 < 8; ++k8) pv[k8] = *(const f32x4*)(smem + LDS_P2 + st * 65536 + ((k8 * 4 + dtl) * 2 + lt) * 1024 + lane * 16);
        const int lrow = 16 * lt + lr, dcol = 16 * dtl + 4 * lg;
        const u16* rq = (const u16*)(smem + LDS_RES + st * 4096 + dcol * 64 + lrow * 2);
        const u32 rp[4] = {rq[0], rq[32], rq[64], rq[96]};
        SCHED();
        const f32x4 s = ((pv[0] + pv[1]) + (pv[2] + pv[3])) + ((pv[4] + pv[5]) + (pv[6] + pv[7]));
        const u32 a0 = cvt2(s[0], s[1]), a1 = cvt2(s[2], s[3]);
        u32x2 o;
        o.x = cvt2(bf2f(rp[0]) + bf2f(a0 & 0xffffu), bf2f(rp[1]) + bfhi(a0));
        o.y = cvt2(bf2f(rp[2]) + bf2f(a1 & 0xffffu), bf2f(rp[3]) + bfhi(a1));
        *(u32x2*)(p.out + (l0 + lrow) * D + 64 * dg + dcol) = o;
      }
    }
  }
  asm volatile("mega_code_end:");
}

extern "C" void kernel_launch(void* const* d_in, const int* in_sizes, int n_in, void* d_out, int out_size, void* d_ws,
                              size_t ws_size, hipStream_t stream) {
  Params p;
  p.x = (const u16*)d_in[0]; p.lar = (const u16*)d_in[1]; p.aim = (const u16*)d_in[2]; p.cr = (const u16*)d_in[3];
  p.ci = (const u16*)d_in[4]; p.ldt = (const u16*)d_in[5]; p.dsk = (const u16*)d_in[6];
  p.wg = (const u16*)d_in[8]; p.wu = (const u16*)d_in[9]; p.wd = (const u16*)d_in[10];
  p.out = (u16*)d_out; p.ws = (char*)d_ws;
  (void)hipFuncSetAttribute((const void*)mega, hipFuncAttributeMaxDynamicSharedMemorySize, LDS_TOTAL);
  mega<<<NWG, NT, LDS_TOTAL, stream>>>(p);
}
```
